```python
import torch

def _softmax(x, axis=-1):
    return torch.softmax(x, dim=axis)
def _gelu(x):
    return torch.nn.functional.gelu(x, approximate="tanh")
def _silu(x):
    return torch.nn.functional.silu(x)
def _sigmoid(x):
    return torch.sigmoid(x)
def _layernorm(x, axis=-1, eps=1e-5):
    m = x.mean(dim=axis, keepdim=True)
    v = x.var(dim=axis, keepdim=True, unbiased=False)
    return (x - m) / torch.sqrt(v + eps)
def _scatter_add(tgt, idx, src, axis=0):
    return tgt.clone().index_add_(axis, idx, src)
def _segment_sum(src, seg, n):
    out = torch.zeros((n,) + src.shape[1:], dtype=src.dtype)
    return out.index_add_(0, seg, src)
def _pad(x, pw):
    flat = tuple(v for pair in reversed(pw) for v in pair)
    return torch.nn.functional.pad(x, flat)

kernel_name = 'sequential_preLN__compressed_kv_cla__glu_gated_geglu__decode'

def setup_inputs():

    _w = (lambda a: (lambda t: t.to(torch.bfloat16) if t.is_floating_point() else t)(torch.as_tensor(a)))
    def _A_setup():
        B, H, Sq, Sk, D, M = 4, 32, 1, 8192, 128, 512
        Dc = 4 * D
        return {
            "x": _w((lambda *s: torch.randn(*s, dtype=torch.bfloat16) * 0.05)(B, Sq, M)),
            "c_kv": _w((lambda *s: torch.randn(*s, dtype=torch.bfloat16) * 0.8)(B, Sk, Dc)),
            "w_q": _w((lambda *s: torch.randn(*s, dtype=torch.bfloat16) * 1.6)(M, H, D) * (M ** -0.5)),
            "w_uk": _w((lambda *s: torch.randn(*s, dtype=torch.bfloat16) * 1.6)(Dc, H, D) * (Dc ** -0.5)),
            "w_uv": _w((lambda *s: torch.randn(*s, dtype=torch.bfloat16) * 0.8)(Dc, H, D) * (Dc ** -0.5)),
            "w_o": _w((lambda *s: torch.randn(*s, dtype=torch.bfloat16) * 0.8)(H, D, M) * ((H * D) ** -0.5)),
        }
    _A_in = _A_setup()

    def _B_setup():
        B, S, Dm, Df = 4, 1, 512, 1376
        return {
            "x": _w((lambda *s: torch.randn(*s, dtype=torch.bfloat16) * 0.05)(B, S, Dm)),
            "w_gate": _w((lambda *s: torch.randn(*s, dtype=torch.bfloat16) * 0.4)(Dm, Df) * (Dm ** -0.5)),
            "w_up": _w((lambda *s: torch.randn(*s, dtype=torch.bfloat16) * 0.4)(Dm, Df) * (Dm ** -0.5)),
            "w_down": _w((lambda *s: torch.randn(*s, dtype=torch.bfloat16) * 0.4)(Df, Dm) * (Df ** -0.5)),
        }
    _B_in = _B_setup()

    return {
        "A_x": _A_in["x"],
        "A_c_kv": _A_in["c_kv"],
        "A_w_q": _A_in["w_q"],
        "A_w_uk": _A_in["w_uk"],
        "A_w_uv": _A_in["w_uv"],
        "A_w_o": _A_in["w_o"],
        "B_x": _B_in["x"],
        "B_w_gate": _B_in["w_gate"],
        "B_w_up": _B_in["w_up"],
        "B_w_down": _B_in["w_down"],
    }

def reference(A_x, A_c_kv, A_w_q, A_w_uk, A_w_uv, A_w_o, B_x, B_w_gate, B_w_up, B_w_down):
    def _A_ref(x, c_kv, w_q, w_uk, w_uv, w_o):
        B, Sq, M = x.shape[0], x.shape[1], x.shape[2]
        Sk = c_kv.shape[1]
        H, D = w_q.shape[1], w_q.shape[2]
        q = torch.einsum("bqm,mhd->bhqd", x, w_q)
        k = torch.einsum("bkc,chd->bhkd", c_kv, w_uk)
        v = torch.einsum("bkc,chd->bhkd", c_kv, w_uv)
        scores = torch.einsum("bhqd,bhkd->bhqk", q, k) * (D ** -0.5)
        off = Sk - Sq
        qi = torch.arange(Sq)[:, None]
        kj = torch.arange(Sk)[None, :]
        cmask = kj <= (qi + off)
        scores = torch.where(cmask[None, None], scores, -1e9)
        p = _softmax(scores, axis=-1)
        ctx = torch.einsum("bhqk,bhkd->bhqd", p, v)
        return torch.einsum("bhqd,hdm->bqm", ctx, w_o)

    def _B_ref(x, w_gate, w_up, w_down):
        g = torch.einsum("bsd,df->bsf", x, w_gate)
        u = torch.einsum("bsd,df->bsf", x, w_up)
        h = _gelu(g) * u
        return torch.einsum("bsf,fd->bsd", h, w_down)

    x = A_x
    h = x + _A_ref(_layernorm(x, axis=-1), A_c_kv, A_w_q, A_w_uk, A_w_uv, A_w_o)
    h2 = h + _B_ref(_layernorm(h, axis=-1), B_w_gate, B_w_up, B_w_down)
    return h2
```

```cpp
#include <hip/hip_runtime.h>
#include <hip/hip_bf16.h>

typedef short s4 __attribute__((ext_vector_type(4)));
typedef __bf16 bf8 __attribute__((ext_vector_type(8)));
typedef __bf16 bf2 __attribute__((ext_vector_type(2)));
typedef float f2 __attribute__((ext_vector_type(2)));
typedef float f4 __attribute__((ext_vector_type(4)));
typedef float f16v __attribute__((ext_vector_type(16)));
typedef unsigned u2 __attribute__((ext_vector_type(2)));
typedef unsigned u4 __attribute__((ext_vector_type(4)));
#define LAS __attribute__((address_space(3)))
#define DI __device__ __forceinline__
typedef __amdgpu_buffer_rsrc_t rsrc_t;

constexpr unsigned OFF_E = 0, OFF_FLAGC = 4096, OFF_QP = 8192, OFF_Q = 0x90000, OFF_ML = 0xB0000, OFF_H = 0xB8000,
                   OFF_HH = 0xBA000, OFF_AP = 0x100000, OFF_OP = 0x200000, OFF_STAMP = 0x800000, WS_USED = 0x900000;
constexpr unsigned SENT = 0xFFFFFFFFu;
constexpr int SPIN_CAP = 200000;
constexpr int AUX_SC1 = 16, AUX_NT = 2;
#define ROLE_OF(b) (((b) >> 3) & 1)

#define STAMP(i)

DI void glds(const void* gsrc, unsigned lds_dst) {
  unsigned keep;
  asm volatile("s_mov_b32 %0, m0\n\ts_mov_b32 m0, %2\n\ts_nop 0\n\tglobal_load_lds_dwordx4 %1, off nt\n\ts_mov_b32 m0, %0"
               : "=&s"(keep) : "v"(gsrc), "s"(lds_dst) : "memory");
}
#define VMWAIT(n) asm volatile("s_waitcnt vmcnt(" #n ")" ::: "memory")
DI void bar() { asm volatile("s_waitcnt lgkmcnt(0)\n\ts_barrier" ::: "memory"); }
DI unsigned pk(float a, float b) { f2 x = {a, b}; bf2 y = __builtin_convertvector(x, bf2); return __builtin_bit_cast(unsigned, y); }
DI unsigned nosent(unsigned v) { return v < 0xFFFFFFFEu ? v : 0xFFFFFFFEu; }
DI float bflo(unsigned v) { return __builtin_bit_cast(float, v << 16); }
DI float bfhi(unsigned v) { return __builtin_bit_cast(float, v & 0xFFFF0000u); }
DI float asf(unsigned v) { return __builtin_bit_cast(float, v); }
DI unsigned asu(float v) { return __builtin_bit_cast(unsigned, v); }
template <int C> DI float dpp(float x) { return asf(__builtin_amdgcn_update_dpp(0u, asu(x), C, 0xF, 0xF, true)); }
DI float add16(float x) { auto s = __builtin_amdgcn_permlane16_swap(asu(x), asu(x), false, false); return asf(s[0]) + asf(s[1]); }
DI float add32(float x) { auto s = __builtin_amdgcn_permlane32_swap(asu(x), asu(x), false, false); return asf(s[0]) + asf(s[1]); }
DI float max16(float x) { auto s = __builtin_amdgcn_permlane16_swap(asu(x), asu(x), false, false); return fmaxf(asf(s[0]), asf(s[1])); }
DI float max32(float x) { auto s = __builtin_amdgcn_permlane32_swap(asu(x), asu(x), false, false); return fmaxf(asf(s[0]), asf(s[1])); }
DI float sum_v(float x) { x += dpp<0x128>(x); x += dpp<0x124>(x); return x; }
DI float sum_vg(float x) { return add32(add16(sum_v(x))); }
DI float sum_all(float x) { x += dpp<0xB1>(x); x += dpp<0x4E>(x); return sum_vg(x); }
DI u4 ld16(rsrc_t r, unsigned off) { return __builtin_amdgcn_raw_buffer_load_b128(r, off, 0, AUX_SC1); }
DI u2 ld8(rsrc_t r, unsigned off) { return __builtin_amdgcn_raw_buffer_load_b64(r, off, 0, AUX_SC1); }
DI void st16(rsrc_t r, unsigned off, u4 v) { __builtin_amdgcn_raw_buffer_store_b128(v, r, off, 0, AUX_SC1); }
DI void st8(rsrc_t r, unsigned off, u2 v) { __builtin_amdgcn_raw_buffer_store_b64(v, r, off, 0, AUX_SC1); }
DI bool ok4(u4 v) { return v.x != SENT && v.y != SENT && v.z != SENT && v.w != SENT; }
DI u4 lrd16(unsigned a) { return *(LAS u4*)a; }
DI void lwr16(unsigned a, u4 v) { *(LAS u4*)a = v; }
DI void lwr8(unsigned a, u2 v) { *(LAS u2*)a = v; }
DI void lwr4(unsigned a, unsigned v) { *(LAS unsigned*)a = v; }
DI float lrdf(unsigned a) { return *(LAS float*)a; }
DI bf8 trfrag(unsigned a0, unsigned a1) {
  s4 x = __builtin_amdgcn_ds_read_tr16_b64_v4i16((LAS s4*)a0), y = __builtin_amdgcn_ds_read_tr16_b64_v4i16((LAS s4*)a1);
  return __builtin_bit_cast(bf8, __builtin_shufflevector(x, y, 0, 1, 2, 3, 4, 5, 6, 7));
}
DI bf8 asb(u4 v) { return __builtin_bit_cast(bf8, v); }
DI f4 mfma16(bf8 a, bf8 b, f4 c) { return __builtin_amdgcn_mfma_f32_16x16x32_bf16(a, b, c, 0, 0, 0); }
DI f16v mfma32(bf8 a, bf8 b, f16v c) { return __builtin_amdgcn_mfma_f32_32x32x16_bf16(a, b, c, 0, 0, 0); }
DI f4 sel4(int v, f4 a, f4 b, f4 c, f4 d) { f4 x = (v & 1) ? b : a, y = (v & 1) ? d : c; return (v & 2) ? y : x; }
DI unsigned wsw(unsigned row) { return ((row & 3) | ((row >> 1) & 4)) << 5; }

struct Params {
  const char *x, *ckv, *wq, *wuk, *wuv, *wo, *wg, *wu, *wd;
  char *out, *ws;
};

DI void wait_flagc(rsrc_t ws, int lane, unsigned elo, unsigned ehi) {
  for (int it = 0; it < SPIN_CAP; ++it) {
        asm volatile("" ::: "memory");
    u4 f = ld16(ws, OFF_FLAGC + lane * 16);
    if (__all(f.x == elo && f.y == ehi && f.z == elo && f.w == ehi)) break;
  }
}

__global__ __launch_bounds__(256, 1) void fused(Params P) {
  const int tid = threadIdx.x, lane = tid & 63, w = __builtin_amdgcn_readfirstlane(tid >> 6);
  const int bid = blockIdx.x, xc = bid & 7, idx = bid >> 4;
  const int n = lane & 15, g = lane >> 4, bb = lane & 3, v = (lane >> 2) & 3;
  const rsrc_t ws = __builtin_amdgcn_make_buffer_rsrc(P.ws, 0, WS_USED, 0x00020000);
  STAMP(0);
  if (ROLE_OF(bid)) {
    const int b = xc >> 1, split = (xc & 1) * 16 + idx;
    const char* cb = P.ckv + ((size_t)b * 8192 + split * 256) * 1024;
    u2 e = ld8(ws, OFF_E);
    auto issue = [&](int r) {
#pragma unroll
      for (int i = 0; i < 16; ++i) glds(cb + (size_t)(r * 64 + w * 16 + i) * 1024 + ((lane ^ i) * 16), (unsigned)(((r & 1) * 64 + w * 16 + i) * 1024));
    };
    issue(0); issue(1);
    const unsigned elo = e.x + 1, ehi = e.y + (elo == 0);
    STAMP(1);
    wait_flagc(ws, lane, elo, ehi);
    STAMP(2);
    constexpr unsigned QL = 131072, PL = QL, MXL = QL + 8192, LSL = QL + 8192 + 1024;
    {
      u4 q[8];
      for (int it = 0; it < SPIN_CAP; ++it) {
        asm volatile("" ::: "memory");
        bool ok = true;
#pragma unroll
        for (int i = 0; i < 8; ++i) { q[i] = ld16(ws, OFF_Q + b * 32768 + (w * 8 + i) * 1024 + lane * 16); }
#pragma unroll
        for (int i = 0; i < 8; ++i) ok &= ok4(q[i]);
        if (__all(ok)) break;
      }
#pragma unroll
      for (int i = 0; i < 8; ++i) lwr16(QL + (w * 8 + i) * 1024 + ((lane ^ ((w * 8 + i) & 15)) * 16), q[i]);
    }
    STAMP(3);
    bar();
    bf8 qf[2][16];
#pragma unroll
    for (int ht = 0; ht < 2; ++ht)
#pragma unroll
      for (int s = 0; s < 16; ++s) qf[ht][s] = asb(lrd16(QL + (ht * 16 + n) * 1024 + g * 256 + ((s ^ n) * 16)));
    bar();
    f16v O[4];
#pragma unroll
    for (int c = 0; c < 4; ++c)
#pragma unroll
      for (int i = 0; i < 16; ++i) O[c][i] = 0.f;
    float m0 = -INFINITY, m1 = -INFINITY, l0 = 0.f, l1 = 0.f;
    const int krow = 4 * (n & 3) + (n >> 2);
    const unsigned qkbase = (w * 16 + krow) * 1024 + g * 256 + krow * 16;
    const int hf = lane >> 5, blk = (lane >> 4) & 1, tq = (lane & 15) >> 2, tp = lane & 3;
    unsigned trb[2];
#pragma unroll
    for (int t = 0; t < 2; ++t) { const int row = 4 * tq + 2 * hf + t; trb[t] = row * 1024 + w * 256 + (((2 * blk + (tp >> 1)) ^ row) * 16) + 8 * (tp & 1); }
#pragma unroll
    for (int r = 0; r < 4; ++r) {
      const unsigned slot = (r & 1) * 65536;
      if (r == 2) VMWAIT(16); else if (r == 3) VMWAIT(0);
      f4 s0 = {0, 0, 0, 0}, s1 = {0, 0, 0, 0};
#pragma unroll
      for (int s = 0; s < 16; ++s) {
        bf8 a = asb(lrd16(slot + (qkbase ^ (s * 16))));
        s0 = mfma16(a, qf[0][s], s0); s1 = mfma16(a, qf[1][s], s1);
      }
      float x0 = max32(max16(fmaxf(fmaxf(s0[0], s0[1]), fmaxf(s0[2], s0[3]))));
      float x1 = max32(max16(fmaxf(fmaxf(s1[0], s1[1]), fmaxf(s1[2], s1[3]))));
      if (g < 2) lwr4(MXL + (lane * 4 + w) * 4, asu(g ? x1 : x0));
      bar();
      u4 ma = lrd16(MXL + n * 16), mb = lrd16(MXL + (16 + n) * 16);
      const float n0 = fmaxf(fmaxf(m0, fmaxf(asf(ma.x), asf(ma.y))), fmaxf(asf(ma.z), asf(ma.w)));
      const float n1 = fmaxf(fmaxf(m1, fmaxf(asf(mb.x), asf(mb.y))), fmaxf(asf(mb.z), asf(mb.w)));
      const float a0 = __builtin_amdgcn_exp2f(m0 - n0), a1 = __builtin_amdgcn_exp2f(m1 - n1);
      m0 = n0; m1 = n1;
      f4 p0, p1;
#pragma unroll
      for (int i = 0; i < 4; ++i) { p0[i] = __builtin_amdgcn_exp2f(s0[i] - n0); p1[i] = __builtin_amdgcn_exp2f(s1[i] - n1); }
      l0 = l0 * a0 + (p0[0] + p0[1]) + (p0[2] + p0[3]);
      l1 = l1 * a1 + (p1[0] + p1[1]) + (p1[2] + p1[3]);
      const unsigned pa = PL + ((w * 2 + (g >> 1)) * 32 + n) * 16 + 8 * (g & 1);
      lwr8(pa, u2{pk(p0[0], p0[1]), pk(p0[2], p0[3])});
      lwr8(pa + 256, u2{pk(p1[0], p1[1]), pk(p1[2], p1[3])});
      if (r > 0) {
        const float al = (g & 1) ? a1 : a0;
#pragma unroll
        for (int c = 0; c < 4; ++c) O[c] *= al;
      }
      bar();
#pragma unroll
      for (int ks = 0; ks < 4; ++ks) {
        bf8 pb = asb(lrd16(PL + (ks * 64 + lane) * 16));
#pragma unroll
        for (int c = 0; c < 4; ++c) {
          bf8 a = trfrag(slot + ks * 16384 + (trb[0] ^ (c * 64)), slot + ks * 16384 + (trb[1] ^ (c * 64)));
          O[c] = mfma32(a, pb, O[c]);
        }
      }
      if (r < 2) { bar(); issue(r + 2); }
      STAMP(4 + r);
    }
    l0 = add32(add16(l0)); l1 = add32(add16(l1));
    if (g < 2) lwr4(LSL + (lane * 4 + w) * 4, asu(g ? l1 : l0));
    const unsigned TL = w * 16384;
#pragma unroll
    for (int c = 0; c < 4; ++c)
#pragma unroll
      for (int i = 0; i < 4; ++i)
        lwr8(TL + (lane & 31) * 272 + (32 * c + 8 * i + 4 * hf) * 2, u2{pk(O[c][4 * i], O[c][4 * i + 1]), pk(O[c][4 * i + 2], O[c][4 * i + 3])});
#pragma unroll
    for (int i = 0; i < 8; ++i) {
      const int h = 4 * i + g;
      st16(ws, OFF_OP + (h * 4 + w) * 32768 + (b * 32 + split) * 256 + n * 16, lrd16(TL + h * 272 + n * 16));
    }
    STAMP(8);
    VMWAIT(0);
    bar();
    if (w == 0 && lane < 32) {
      u4 ls = lrd16(LSL + lane * 16);
      const float L = (asf(ls.x) + asf(ls.y)) + (asf(ls.z) + asf(ls.w));
      st8(ws, OFF_ML + (b * 32 + split) * 256 + lane * 8, u2{nosent(asu(g ? m1 : m0)), nosent(asu(L))});
    }
    STAMP(9);
  } else {
    const int h = xc * 4 + (idx >> 2), j = idx & 3, cu = h * 4 + j;
    const int fc = (idx >= 4 && idx < 10 && xc * 6 + idx - 4 < 43) ? xc * 6 + idx - 4 : -1;
    const int dc = idx < 4 ? xc * 4 + idx : -1;
    constexpr unsigned SCR = 159744, XN = SCR, QB = SCR + 1024, CX = SCR + 2048, RD = SCR + 3072;
    u2 e = ld8(ws, OFF_E);
    u4 xr = *(const u4*)(P.x + w * 1024 + lane * 16);
    u2 xres = *(const u2*)(P.x + bb * 1024 + cu * 8);
    const u4 S4 = {SENT, SENT, SENT, SENT};
    st16(ws, OFF_AP + cu * 8192 + tid * 16, S4);
    st16(ws, OFF_AP + cu * 8192 + 4096 + tid * 16, S4);
    {
      unsigned off; bool on = true;
      if (w == 0) off = OFF_Q + g * 32768 + h * 1024 + j * 256 + n * 16;
      else if (w == 1) { off = OFF_ML + cu * 256 + lane * 16; on = lane < 16; }
      else if (w == 2) { off = OFF_H + cu * 64 + lane * 16; on = lane < 4; }
      else { off = OFF_HH + fc * 256 + lane * 16; on = lane < 16 && fc >= 0; }
      if (on) st16(ws, off, S4);
    }
    asm volatile("" ::: "memory");
#pragma unroll
    for (int i = 0; i < 8; ++i) {
      const int row = w * 32 + i * 4 + g;
      glds(P.wq + (size_t)(j * 128 + row) * 8192 + h * 256 + ((n * 16) ^ wsw(row)), (unsigned)((w * 32 + i * 4) * 256));
    }
    u4 uk[2][4];
#pragma unroll
    for (int t = 0; t < 2; ++t)
#pragma unroll
      for (int s = 0; s < 4; ++s)
        uk[t][s] = __builtin_nontemporal_load((const u4*)(P.wuk + (size_t)(j * 128 + w * 32 + 8 * (n >> 2) + 4 * t + (n & 3)) * 8192 + h * 256 + s * 64 + g * 16));
    asm volatile("" ::: "memory");
    VMWAIT(16);
    {
      float xv[8] = {bflo(xr.x), bfhi(xr.x), bflo(xr.y), bfhi(xr.y), bflo(xr.z), bfhi(xr.z), bflo(xr.w), bfhi(xr.w)};
      float s = 0;
#pragma unroll
      for (int i = 0; i < 8; ++i) s += xv[i];
      const float mean = sum_all(s) * (1.f / 512.f);
      float q2 = 0;
#pragma unroll
      for (int i = 0; i < 8; ++i) { xv[i] -= mean; q2 += xv[i] * xv[i]; }
      const float rs = __builtin_amdgcn_rsqf(sum_all(q2) * (1.f / 512.f) + 1e-5f);
      if (g == j) lwr16(XN + w * 256 + n * 16, u4{pk(xv[0] * rs, xv[1] * rs), pk(xv[2] * rs, xv[3] * rs), pk(xv[4] * rs, xv[5] * rs), pk(xv[6] * rs, xv[7] * rs)});
    }
    const unsigned elo = e.x + 1, ehi = e.y + (elo == 0);
    bar();
    if (tid == 0) st8(ws, OFF_FLAGC + cu * 8, u2{elo, ehi});
    STAMP(1);
    VMWAIT(8);
    bar();
    STAMP(2);
    const int tq = (lane & 15) >> 2, tp = lane & 3;
    {
      f4 a0 = {0, 0, 0, 0}, a1 = {0, 0, 0, 0};
#pragma unroll
      for (int s = 0; s < 4; ++s) {
        bf8 xb = asb(lrd16(XN + bb * 256 + s * 64 + g * 16));
        const int r0 = 32 * s + 8 * g + tq, r1 = r0 + 4;
        a0 = mfma16(trfrag(r0 * 256 + ((w * 64 + tp * 8) ^ wsw(r0)), r1 * 256 + ((w * 64 + tp * 8) ^ wsw(r1))), xb, a0);
        a1 = mfma16(trfrag(r0 * 256 + ((w * 64 + 32 + tp * 8) ^ wsw(r0)), r1 * 256 + ((w * 64 + 32 + tp * 8) ^ wsw(r1))), xb, a1);
      }
      f4 a = (v & 1) ? a1 : a0;
      const unsigned off = OFF_QP + cu * 4096 + (bb * 128 + w * 32 + (v & 1) * 16 + 4 * g + (v & 2)) * 8;
      st16(ws, off, (v & 2) ? u4{asu(a[2]), elo, asu(a[3]), elo} : u4{asu(a[0]), elo, asu(a[1]), elo});
    }
    STAMP(3);
    {
      u4 t[4];
      for (int it = 0; it < SPIN_CAP; ++it) {
        asm volatile("" ::: "memory");
        bool ok = true;
#pragma unroll
        for (int i = 0; i < 4; ++i) t[i] = ld16(ws, OFF_QP + (h * 4 + i) * 4096 + tid * 16);
#pragma unroll
        for (int i = 0; i < 4; ++i) ok &= (t[i].y == elo && t[i].w == elo);
        if (__all(ok)) break;
      }
      lwr4(QB + tid * 4, pk((asf(t[0].x) + asf(t[1].x)) + (asf(t[2].x) + asf(t[3].x)), (asf(t[0].z) + asf(t[1].z)) + (asf(t[2].z) + asf(t[3].z))));
    }
    bar();
    STAMP(4);
    {
      f4 a0 = {0, 0, 0, 0}, a1 = {0, 0, 0, 0};
#pragma unroll
      for (int s = 0; s < 4; ++s) {
        bf8 qb = asb(lrd16(QB + bb * 256 + s * 64 + g * 16));
        a0 = mfma16(asb(uk[0][s]), qb, a0); a1 = mfma16(asb(uk[1][s]), qb, a1);
      }
      const float sc = 0.12751721f;
      if (v == 0) st16(ws, OFF_Q + bb * 32768 + h * 1024 + j * 256 + w * 64 + g * 16,
                       u4{nosent(pk(a0[0] * sc, a0[1] * sc)), nosent(pk(a0[2] * sc, a0[3] * sc)), nosent(pk(a1[0] * sc, a1[1] * sc)), nosent(pk(a1[2] * sc, a1[3] * sc))});
    }
    STAMP(5);
#pragma unroll
    for (int i = 0; i < 8; ++i) {
      const int row = w * 32 + i * 4 + g;
      glds(P.wuv + (size_t)(j * 128 + row) * 8192 + h * 256 + ((n * 16) ^ wsw(row)), (unsigned)(32768 + (w * 32 + i * 4) * 256));
    }
    if (fc >= 0) {
      const char* src = ((lane & 4) ? P.wu : P.wg) + fc * 64 + (lane & 3) * 16;
#pragma unroll
      for (int i = 0; i < 16; ++i) glds(src + (size_t)(w * 128 + i * 8 + (lane >> 3)) * 2752, (unsigned)(65536 + (w * 128 + i * 8) * 128));
    } else if (dc >= 0) {
      for (int i = w; i < 43; i += 4) glds(P.wd + (size_t)(i * 32 + (lane >> 1)) * 1024 + dc * 32 + (lane & 1) * 16, (unsigned)(65536 + i * 1024));
    }
    wait_flagc(ws, lane, elo, ehi);
    VMWAIT(0);
    bar();
    bf8 uv[2][4], ff[16];
#pragma unroll
    for (int t = 0; t < 2; ++t)
#pragma unroll
      for (int s = 0; s < 4; ++s) {
        const int r0 = 32 * s + 8 * g + tq, r1 = r0 + 4;
        uv[t][s] = trfrag(32768 + r0 * 256 + ((w * 64 + t * 32 + tp * 8) ^ wsw(r0)), 32768 + r1 * 256 + ((w * 64 + t * 32 + tp * 8) ^ wsw(r1)));
      }
    {
      if (fc >= 0) {
        const unsigned a0 = 65536 + (16 * g + tq) * 128 + (tp >> 1) * 64 + w * 16 + (tp & 1) * 8;
#pragma unroll
        for (int k = 0; k < 16; ++k) ff[k] = trfrag(a0 + (128 * (k >> 2) + 4 * (k & 3)) * 128, a0 + (128 * (k >> 2) + 4 * (k & 3) + 64) * 128);
      } else {
#pragma unroll
        for (int k = 0; k < 16; ++k) {
          int row = 8 * (16 * (3 * w + (k >> 2)) + 4 * g + (k & 3)) + tq; row = row > 1371 ? 1368 : row;
          ff[k] = trfrag(65536 + row * 32 + tp * 8, 65536 + row * 32 + tp * 8 + 128);
        }
      }
    }
    bar();
#pragma unroll
    for (int i = 0; i < 32; ++i) {
      const int row = w * 32 + i;
      glds(P.wo + (size_t)(h * 128 + row) * 1024 + ((lane * 16) ^ wsw(row)), (unsigned)(row * 1024));
    }
    VMWAIT(0);
    bar();
    bf8 wo[8][4];
#pragma unroll
    for (int t = 0; t < 8; ++t)
#pragma unroll
      for (int s = 0; s < 4; ++s) {
        const int r0 = 32 * s + 8 * g + tq, r1 = r0 + 4;
        wo[t][s] = trfrag(r0 * 1024 + ((w * 256 + t * 32 + tp * 8) ^ wsw(r0)), r1 * 1024 + ((w * 256 + t * 32 + tp * 8) ^ wsw(r1)));
      }
    asm volatile("s_waitcnt lgkmcnt(0)" ::: "memory");
    STAMP(6);
    {
      u2 ml;
      for (int it = 0; it < SPIN_CAP; ++it) {
        asm volatile("" ::: "memory");
        ml = ld8(ws, OFF_ML + (w * 32 + (lane & 31)) * 256 + h * 8);
        if (__all(ml.x != SENT && ml.y != SENT)) break;
      }
      asm volatile("" ::: "memory");
      STAMP(7);
      u4 o[8];
#pragma unroll
      for (int i = 0; i < 8; ++i) o[i] = ld16(ws, OFF_OP + cu * 32768 + w * 8192 + i * 1024 + lane * 16);
      float mx = asf(ml.x);
      mx = fmaxf(mx, dpp<0xB1>(mx)); mx = fmaxf(mx, dpp<0x4E>(mx)); mx = fmaxf(mx, dpp<0x124>(mx)); mx = fmaxf(mx, dpp<0x128>(mx)); mx = max16(mx);
      const float ex = __builtin_amdgcn_exp2f(asf(ml.x) - mx);
      float L = ex * asf(ml.y);
      L += dpp<0xB1>(L); L += dpp<0x4E>(L); L = add16(sum_v(L));
      const float wt = ex * __builtin_amdgcn_rcpf(L);
      float acc[8] = {0, 0, 0, 0, 0, 0, 0, 0};
#pragma unroll
      for (int i = 0; i < 8; ++i) {
        const float ws_ = asf(__builtin_amdgcn_ds_bpermute((4 * i + g) * 4, asu(wt)));
        acc[0] += ws_ * bflo(o[i].x); acc[1] += ws_ * bfhi(o[i].x); acc[2] += ws_ * bflo(o[i].y); acc[3] += ws_ * bfhi(o[i].y);
        acc[4] += ws_ * bflo(o[i].z); acc[5] += ws_ * bfhi(o[i].z); acc[6] += ws_ * bflo(o[i].w); acc[7] += ws_ * bfhi(o[i].w);
      }
#pragma unroll
      for (int i = 0; i < 8; ++i) acc[i] = add32(add16(acc[i]));
      if (g == 0) lwr16(XN + w * 256 + n * 16, u4{pk(acc[0], acc[1]), pk(acc[2], acc[3]), pk(acc[4], acc[5]), pk(acc[6], acc[7])});
    }
    bar();
    STAMP(8);
    if (cu == 0 && tid == 0) st8(ws, OFF_E, u2{elo, ehi});
    {
      f4 a0 = {0, 0, 0, 0}, a1 = {0, 0, 0, 0};
#pragma unroll
      for (int s = 0; s < 4; ++s) {
        bf8 cbv = asb(lrd16(XN + bb * 256 + s * 64 + g * 16));
        a0 = mfma16(uv[0][s], cbv, a0); a1 = mfma16(uv[1][s], cbv, a1);
      }
      f4 a = (v & 1) ? a1 : a0;
      if (v < 2) lwr8(CX + bb * 256 + (w * 32 + v * 16 + 4 * g) * 2, u2{pk(a[0], a[1]), pk(a[2], a[3])});
    }
    bar();
    {
      bf8 cbv[4];
#pragma unroll
      for (int s = 0; s < 4; ++s) cbv[s] = asb(lrd16(CX + bb * 256 + s * 64 + g * 16));
#pragma unroll
      for (int hh = 0; hh < 2; ++hh) {
        f4 a[4];
#pragma unroll
        for (int t = 0; t < 4; ++t) {
          a[t] = f4{0, 0, 0, 0};
#pragma unroll
          for (int s = 0; s < 4; ++s) a[t] = mfma16(wo[hh * 4 + t][s], cbv[s], a[t]);
        }
        f4 r = sel4(v, a[0], a[1], a[2], a[3]);
        const int red = w * 32 + hh * 16 + v * 4 + g;
        st16(ws, OFF_AP + red * 8192 + cu * 64 + bb * 16, u4{nosent(asu(r[0])), nosent(asu(r[1])), nosent(asu(r[2])), nosent(asu(r[3]))});
      }
    }
    STAMP(9);
    if (w == 3) {
      u4 t[8];
      for (int it = 0; it < SPIN_CAP; ++it) {
        asm volatile("" ::: "memory");
        bool ok = true;
#pragma unroll
        for (int i = 0; i < 8; ++i) t[i] = ld16(ws, OFF_AP + cu * 8192 + i * 1024 + lane * 16);
#pragma unroll
        for (int i = 0; i < 8; ++i) ok &= ok4(t[i]);
        if (__all(ok)) break;
      }
      f4 s = {0, 0, 0, 0};
#pragma unroll
      for (int i = 0; i < 8; ++i) s += __builtin_bit_cast(f4, t[i]);
      const float h0 = sum_vg(s[0]) + bflo(xres.x), h1 = sum_vg(s[1]) + bfhi(xres.x), h2 = sum_vg(s[2]) + bflo(xres.y), h3 = sum_vg(s[3]) + bfhi(xres.y);
      if (lane < 4) st16(ws, OFF_H + cu * 64 + lane * 16, u4{nosent(asu(h0)), nosent(asu(h1)), nosent(asu(h2)), nosent(asu(h3))});
    }
    STAMP(10);
    if (fc >= 0) {
      u4 t[8];
      for (int it = 0; it < SPIN_CAP; ++it) {
        asm volatile("" ::: "memory");
        bool ok = true;
#pragma unroll
        for (int i = 0; i < 8; ++i) t[i] = ld16(ws, OFF_H + i * 1024 + lane * 16);
#pragma unroll
        for (int i = 0; i < 8; ++i) ok &= ok4(t[i]);
        if (__all(ok)) break;
      }
      STAMP(11);
      f4 hv[8]; f4 s4v = {0, 0, 0, 0};
#pragma unroll
      for (int i = 0; i < 8; ++i) { hv[i] = __builtin_bit_cast(f4, t[i]); s4v += hv[i]; }
      const float mean = sum_vg((s4v[0] + s4v[1]) + (s4v[2] + s4v[3])) * (1.f / 512.f);
      f4 q4 = {0, 0, 0, 0};
#pragma unroll
      for (int i = 0; i < 8; ++i) { hv[i] -= mean; q4 += hv[i] * hv[i]; }
      const float rs = __builtin_amdgcn_rsqf(sum_vg((q4[0] + q4[1]) + (q4[2] + q4[3])) * (1.f / 512.f) + 1e-5f);
      f4 a[4] = {{0, 0, 0, 0}, {0, 0, 0, 0}, {0, 0, 0, 0}, {0, 0, 0, 0}};
#pragma unroll
      for (int u = 0; u < 4; ++u) {
        const f4 y0 = hv[2 * u] * rs, y1 = hv[2 * u + 1] * rs;
        const bf8 hb = asb(u4{pk(y0[0], y0[1]), pk(y0[2], y0[3]), pk(y1[0], y1[1]), pk(y1[2], y1[3])});
#pragma unroll
        for (int vv = 0; vv < 4; ++vv) a[vv] = mfma16(ff[u * 4 + vv], hb, a[vv]);
      }
      f4 r = sel4(v, a[0], a[1], a[2], a[3]);
      float hh[4];
#pragma unroll
      for (int i = 0; i < 4; ++i) {
        const float z = sum_v(r[i]);
        auto sw = __builtin_amdgcn_permlane32_swap(asu(z), asu(z), false, false);
        const float gt = asf(sw[0]), up = asf(sw[1]);
        const float inner = 0.7978845608f * (gt + 0.044715f * gt * gt * gt);
        hh[i] = gt * __builtin_amdgcn_rcpf(1.f + __builtin_amdgcn_exp2f(-2.885390082f * inner)) * up;
      }
      if (v == 0 && g < 2) st8(ws, OFF_HH + (fc * 4 + w) * 64 + bb * 16 + g * 8, u2{nosent(pk(hh[0], hh[1])), nosent(pk(hh[2], hh[3]))});
      STAMP(12);
    } else if (dc >= 0) {
      u4 hr;
      for (int it = 0; it < SPIN_CAP; ++it) {
        asm volatile("" ::: "memory");
        hr = ld16(ws, OFF_H + (dc * 4 + g) * 64 + bb * 16);
        if (__all(ok4(hr))) break;
      }
      STAMP(11);
      u4 t[3];
      for (int it = 0; it < SPIN_CAP; ++it) {
        asm volatile("" ::: "memory");
        bool ok = true;
#pragma unroll
        for (int i = 0; i < 3; ++i) t[i] = ld16(ws, OFF_HH + (3 * w + i) * 1024 + lane * 16);
#pragma unroll
        for (int i = 0; i < 3; ++i) { const bool live = (3 * w + i) * 16 + (lane >> 2) < 172; ok &= (!live || ok4(t[i])); if (!live) t[i] = u4{0, 0, 0, 0}; }
        if (__all(ok)) break;
      }
      STAMP(13);
      f4 a[4] = {{0, 0, 0, 0}, {0, 0, 0, 0}, {0, 0, 0, 0}, {0, 0, 0, 0}};
#pragma unroll
      for (int i = 0; i < 3; ++i)
#pragma unroll
        for (int vv = 0; vv < 4; ++vv) a[vv] = mfma16(ff[i * 4 + vv], asb(t[i]), a[vv]);
      f4 r = sel4(v, a[0], a[1], a[2], a[3]);
#pragma unroll
      for (int i = 0; i < 4; ++i) r[i] = sum_v(r[i]);
      if (v == 0) lwr16(RD + w * 256 + (g * 4 + bb) * 16, __builtin_bit_cast(u4, r));
      bar();
      if (w == 0 && v == 0) {
        f4 o = __builtin_bit_cast(f4, hr);
#pragma unroll
        for (int i = 0; i < 4; ++i) o += __builtin_bit_cast(f4, lrd16(RD + i * 256 + (g * 4 + bb) * 16));
        *(u2*)(P.out + bb * 1024 + dc * 32 + g * 8) = u2{pk(o[0], o[1]), pk(o[2], o[3])};
      }
      STAMP(14);
    }
  }
}

extern "C" void kernel_launch(void* const* d_in, const int* in_sizes, int n_in, void* d_out, int out_size, void* d_ws, size_t ws_size,
                              hipStream_t stream) {
  Params p;
  p.x = (const char*)d_in[0]; p.ckv = (const char*)d_in[1]; p.wq = (const char*)d_in[2]; p.wuk = (const char*)d_in[3];
  p.wuv = (const char*)d_in[4]; p.wo = (const char*)d_in[5]; p.wg = (const char*)d_in[7]; p.wu = (const char*)d_in[8];
  p.wd = (const char*)d_in[9]; p.out = (char*)d_out; p.ws = (char*)d_ws;
  (void)hipFuncSetAttribute((const void*)fused, hipFuncAttributeMaxDynamicSharedMemorySize, 163840);
  fused<<<256, 256, 163840, stream>>>(p);
}
```

```cpp
#include <hip/hip_runtime.h>
#include <hip/hip_bf16.h>

typedef short s4 __attribute__((ext_vector_type(4)));
typedef __bf16 bf8 __attribute__((ext_vector_type(8)));
typedef __bf16 bf2 __attribute__((ext_vector_type(2)));
typedef float f2 __attribute__((ext_vector_type(2)));
typedef float f4 __attribute__((ext_vector_type(4)));
typedef float f16v __attribute__((ext_vector_type(16)));
typedef unsigned u2 __attribute__((ext_vector_type(2)));
typedef unsigned u4 __attribute__((ext_vector_type(4)));
#define LAS __attribute__((address_space(3)))
#define DI __device__ __forceinline__
typedef __amdgpu_buffer_rsrc_t rsrc_t;

constexpr unsigned OFF_E = 0, OFF_FLAGC = 0x1000, OFF_QP = 0x10000, OFF_Q = 0x90000, OFF_ML = 0xB0000, OFF_H = 0xC0000,
                   OFF_HH = 0xE0000, OFF_AP = 0x100000, OFF_OP = 0x200000, OFF_STAMP = 0x800000, WS_USED = 0x900000;
constexpr unsigned SENT = 0xFFFFFFFFu;
constexpr int SPIN_CAP = 200000;
constexpr int AUX_SC1 = 16, AUX_NT = 2;
#define RH 8
#define RHH 8
#define ATT_DELAY 0
#define TAIL_DELAY 0
#define HH_DELAY 24
#define FENCE() asm volatile("" ::: "memory")
#define SLEEP(n) __builtin_amdgcn_s_sleep(n)
#define ROLE_OF(b) (((b) >> 3) & 1)

#define STAMP(i)

DI void glds(const void* gsrc, unsigned lds_dst) {
  unsigned keep;
  asm volatile("s_mov_b32 %0, m0\n\ts_mov_b32 m0, %2\n\ts_nop 0\n\tglobal_load_lds_dwordx4 %1, off nt\n\ts_mov_b32 m0, %0"
               : "=&s"(keep) : "v"(gsrc), "s"(lds_dst) : "memory");
}
#define VMWAIT(n) asm volatile("s_waitcnt vmcnt(" #n ")" ::: "memory")
DI void bar() { asm volatile("s_waitcnt lgkmcnt(0)\n\ts_barrier" ::: "memory"); }
DI unsigned pk(float a, float b) { f2 x = {a, b}; bf2 y = __builtin_convertvector(x, bf2); return __builtin_bit_cast(unsigned, y); }
DI unsigned nosent(unsigned v) { return v < 0xFFFFFFFEu ? v : 0xFFFFFFFEu; }
DI float bflo(unsigned v) { return __builtin_bit_cast(float, v << 16); }
DI float bfhi(unsigned v) { return __builtin_bit_cast(float, v & 0xFFFF0000u); }
DI float asf(unsigned v) { return __builtin_bit_cast(float, v); }
DI unsigned asu(float v) { return __builtin_bit_cast(unsigned, v); }
template <int C> DI float dpp(float x) { return asf(__builtin_amdgcn_update_dpp(0u, asu(x), C, 0xF, 0xF, true)); }
DI float add16(float x) { auto s = __builtin_amdgcn_permlane16_swap(asu(x), asu(x), false, false); return asf(s[0]) + asf(s[1]); }
DI float add32(float x) { auto s = __builtin_amdgcn_permlane32_swap(asu(x), asu(x), false, false); return asf(s[0]) + asf(s[1]); }
DI float max16(float x) { auto s = __builtin_amdgcn_permlane16_swap(asu(x), asu(x), false, false); return fmaxf(asf(s[0]), asf(s[1])); }
DI float max32(float x) { auto s = __builtin_amdgcn_permlane32_swap(asu(x), asu(x), false, false); return fmaxf(asf(s[0]), asf(s[1])); }
DI float sum_v(float x) { x += dpp<0x128>(x); x += dpp<0x124>(x); return x; }
DI float sum_vg(float x) { return add32(add16(sum_v(x))); }
DI float sum_all(float x) { x += dpp<0xB1>(x); x += dpp<0x4E>(x); return sum_vg(x); }
DI u4 ld16(rsrc_t r, unsigned off) { return __builtin_amdgcn_raw_buffer_load_b128(r, off, 0, AUX_SC1); }
DI u2 ld8(rsrc_t r, unsigned off) { return __builtin_amdgcn_raw_buffer_load_b64(r, off, 0, AUX_SC1); }
DI void st16(rsrc_t r, unsigned off, u4 v) { __builtin_amdgcn_raw_buffer_store_b128(v, r, off, 0, AUX_SC1); }
DI void st8(rsrc_t r, unsigned off, u2 v) { __builtin_amdgcn_raw_buffer_store_b64(v, r, off, 0, AUX_SC1); }
DI bool ok4(u4 v) { return v.x != SENT && v.y != SENT && v.z != SENT && v.w != SENT; }
DI u4 lrd16(unsigned a) { return *(LAS u4*)a; }
DI void lwr16(unsigned a, u4 v) { *(LAS u4*)a = v; }
DI void lwr8(unsigned a, u2 v) { *(LAS u2*)a = v; }
DI void lwr4(unsigned a, unsigned v) { *(LAS unsigned*)a = v; }
DI float lrdf(unsigned a) { return *(LAS float*)a; }
DI bf8 trfrag(unsigned a0, unsigned a1) {
  s4 x = __builtin_amdgcn_ds_read_tr16_b64_v4i16((LAS s4*)a0), y = __builtin_amdgcn_ds_read_tr16_b64_v4i16((LAS s4*)a1);
  return __builtin_bit_cast(bf8, __builtin_shufflevector(x, y, 0, 1, 2, 3, 4, 5, 6, 7));
}
DI bf8 asb(u4 v) { return __builtin_bit_cast(bf8, v); }
DI f4 mfma16(bf8 a, bf8 b, f4 c) { return __builtin_amdgcn_mfma_f32_16x16x32_bf16(a, b, c, 0, 0, 0); }
DI f16v mfma32(bf8 a, bf8 b, f16v c) { return __builtin_amdgcn_mfma_f32_32x32x16_bf16(a, b, c, 0, 0, 0); }
DI f4 sel4(int v, f4 a, f4 b, f4 c, f4 d) { f4 x = (v & 1) ? b : a, y = (v & 1) ? d : c; return (v & 2) ? y : x; }
DI unsigned wsw(unsigned row) { return ((row & 3) | ((row >> 1) & 4)) << 5; }

struct Params {
  const char *x, *ckv, *wq, *wuk, *wuv, *wo, *wg, *wu, *wd;
  char *out, *ws;
};

DI void wait_flagc(rsrc_t ws, int lane, int rep, unsigned elo, unsigned ehi) {
  for (int it = 0; it < SPIN_CAP; ++it) {
    FENCE();
    u4 f = ld16(ws, OFF_FLAGC + rep * 1024 + lane * 16);
    if (__all(f.x == elo && f.y == ehi && f.z == elo && f.w == ehi)) break;
    SLEEP(8);
  }
  FENCE();
}

__global__ __launch_bounds__(512, 2) void fused(Params P) {
  const int tid = threadIdx.x, lane = tid & 63, w = __builtin_amdgcn_readfirstlane(tid >> 6);
  const int bid = blockIdx.x, xc = bid & 7, idx = bid >> 4;
  const int n = lane & 15, g = lane >> 4, bb = lane & 3, v = (lane >> 2) & 3;
  const int tq = (lane & 15) >> 2, tp = lane & 3;
  const rsrc_t ws = __builtin_amdgcn_make_buffer_rsrc(P.ws, 0, WS_USED, 0x00020000);
  STAMP(0);
  if (ROLE_OF(bid)) {
    const int b = xc >> 1, split = (xc & 1) * 16 + idx;
    const char* cb = P.ckv + ((size_t)b * 8192 + split * 256) * 1024;
    constexpr unsigned QL = 131072, PL = QL, MXL = QL + 8192, LSL = QL + 8192 + 1024;
    constexpr float TH = 40.f;
    if (w < 4) {
      u2 e = ld8(ws, OFF_E);
      if (ATT_DELAY) SLEEP(ATT_DELAY);
#pragma unroll
      for (int i = 0; i < 32; ++i) { const int row = (i >> 4) * 64 + w * 16 + (i & 15); glds(cb + (size_t)row * 1024 + ((lane ^ (i & 15)) * 16), (unsigned)(row * 1024)); }
      bar();
      STAMP(1);
      const unsigned elo = e.x + 1, ehi = e.y + (elo == 0);
      wait_flagc(ws, lane, xc, elo, ehi);
      STAMP(2);
      {
        u4 q[8];
        for (int it = 0; it < SPIN_CAP; ++it) {
          FENCE();
          bool ok = true;
#pragma unroll
          for (int i = 0; i < 8; ++i) q[i] = ld16(ws, OFF_Q + b * 32768 + (w * 8 + i) * 1024 + lane * 16);
#pragma unroll
          for (int i = 0; i < 8; ++i) ok &= ok4(q[i]);
          if (__all(ok)) break;
        }
#pragma unroll
        for (int i = 0; i < 8; ++i) lwr16(QL + (w * 8 + i) * 1024 + ((lane ^ ((w * 8 + i) & 15)) * 16), q[i]);
      }
      STAMP(3);
      bar();
      bf8 qf[2][16];
#pragma unroll
      for (int ht = 0; ht < 2; ++ht)
#pragma unroll
        for (int s = 0; s < 16; ++s) qf[ht][s] = asb(lrd16(QL + (ht * 16 + n) * 1024 + g * 256 + ((s ^ n) * 16)));
      bar();
      STAMP(4);
      float m0 = -INFINITY, m1 = -INFINITY, l0 = 0.f, l1 = 0.f;
      const int krow = 4 * (n & 3) + (n >> 2);
      unsigned qkb = (w * 16 + krow) * 1024 + g * 256 + krow * 16;
      unsigned pa = PL + ((w * 2 + (g >> 1)) * 32 + n) * 16 + 8 * (g & 1), mxa = MXL;
#pragma nounroll
      for (int r = 0; r < 4; ++r) {
        bf8 a[16];
#pragma unroll
        for (int s = 0; s < 16; ++s) a[s] = asb(lrd16(qkb ^ (s * 16)));
        __builtin_amdgcn_sched_barrier(0);
        f4 s0 = {0, 0, 0, 0}, s1 = {0, 0, 0, 0};
#pragma unroll
        for (int s = 0; s < 16; ++s) { s0 = mfma16(a[s], qf[0][s], s0); s1 = mfma16(a[s], qf[1][s], s1); }
        const float x0 = max32(max16(fmaxf(fmaxf(s0[0], s0[1]), fmaxf(s0[2], s0[3]))));
        const float x1 = max32(max16(fmaxf(fmaxf(s1[0], s1[1]), fmaxf(s1[2], s1[3]))));
        if (g < 2) lwr4(mxa + (lane * 4 + w) * 4, asu(g ? x1 : x0));
        float t0, t1;
        auto emit = [&]() {
          f4 p0, p1;
#pragma unroll
          for (int i = 0; i < 4; ++i) { p0[i] = __builtin_amdgcn_exp2f(s0[i] - m0); p1[i] = __builtin_amdgcn_exp2f(s1[i] - m1); }
          t0 = (p0[0] + p0[1]) + (p0[2] + p0[3]); t1 = (p1[0] + p1[1]) + (p1[2] + p1[3]);
          lwr8(pa, u2{pk(p0[0], p0[1]), pk(p0[2], p0[3])});
          lwr8(pa + 256, u2{pk(p1[0], p1[1]), pk(p1[2], p1[3])});
        };
        emit();
        bar();
        const u4 ma = lrd16(mxa + n * 16), mb = lrd16(mxa + (16 + n) * 16);
        const float n0 = fmaxf(fmaxf(asf(ma.x), asf(ma.y)), fmaxf(asf(ma.z), asf(ma.w)));
        const float n1 = fmaxf(fmaxf(asf(mb.x), asf(mb.y)), fmaxf(asf(mb.z), asf(mb.w)));
        if (__any(n0 > m0 + TH || n1 > m1 + TH)) {
          const float k0 = fmaxf(m0, n0), k1 = fmaxf(m1, n1);
          l0 *= __builtin_amdgcn_exp2f(m0 - k0); l1 *= __builtin_amdgcn_exp2f(m1 - k1);
          m0 = k0; m1 = k1;
          emit();
          bar();
        }
        l0 += t0; l1 += t1;
        qkb ^= 65536; pa ^= 4096; mxa ^= 512;
        STAMP(5 + r);
      }
      l0 = add32(add16(l0)); l1 = add32(add16(l1));
      if (g < 2) lwr4(LSL + (lane * 4 + w) * 4, asu(g ? l1 : l0));
      bar();
      if (w == 0 && lane < 32) {
        const u4 ls = lrd16(LSL + lane * 16);
        const float L = (asf(ls.x) + asf(ls.y)) + (asf(ls.z) + asf(ls.w));
        st8(ws, OFF_ML + lane * 1024 + (b * 32 + split) * 8, u2{nosent(asu(g ? m1 : m0)), nosent(asu(L))});
      }
      STAMP(10);
    } else {
      const int pw = w - 4, hf = lane >> 5, blk = g & 1;
      bar();
      u4 Ld[32];
#pragma unroll
      for (int i = 0; i < 32; ++i) Ld[i] = __builtin_nontemporal_load((const u4*)(cb + (size_t)(128 + 4 * i + g) * 1024 + pw * 256 + n * 16));
      STAMP(1);
      bar();
      bar();
      f16v O[4];
#pragma unroll
      for (int c = 0; c < 4; ++c)
#pragma unroll
        for (int i = 0; i < 16; ++i) O[c][i] = 0.f;
      float mr = -INFINITY;
      unsigned trb[2];
#pragma unroll
      for (int t = 0; t < 2; ++t) { const int row = 4 * tq + 2 * hf + t; trb[t] = row * 1024 + pw * 256 + (((2 * blk + (tp >> 1)) ^ row) * 16) + 8 * (tp & 1); }
      const unsigned wb = g * 1024 + pw * 256 + ((n ^ g) * 16);
#pragma unroll
      for (int r = 0; r < 4; ++r) {
        bar();
        const u4 ma = lrd16(MXL + (r & 1) * 512 + (lane & 31) * 16);
        const float n0 = fmaxf(fmaxf(asf(ma.x), asf(ma.y)), fmaxf(asf(ma.z), asf(ma.w)));
        if (__any(n0 > mr + TH)) {
          const float k0 = fmaxf(mr, n0), al = __builtin_amdgcn_exp2f(mr - k0);
          mr = k0;
          if (r > 0) {
#pragma unroll
            for (int c = 0; c < 4; ++c) O[c] *= al;
          }
          bar();
        }
        const unsigned slot = (r & 1) * 65536;
#pragma unroll
        for (int ks = 0; ks < 4; ++ks) {
          const bf8 pb = asb(lrd16(PL + (r & 1) * 4096 + (ks * 64 + lane) * 16));
          bf8 a[4];
#pragma unroll
          for (int c = 0; c < 4; ++c) a[c] = trfrag(slot + ks * 16384 + (trb[0] ^ (c * 64)), slot + ks * 16384 + (trb[1] ^ (c * 64)));
          if (r < 2) {
#pragma unroll
            for (int i = 0; i < 4; ++i) lwr16(slot + (ks * 16 + 4 * i) * 1024 + (wb ^ (i * 64)), Ld[r * 16 + ks * 4 + i]);
          }
#pragma unroll
          for (int c = 0; c < 4; ++c) O[c] = mfma32(a[c], pb, O[c]);
        }
        STAMP(5 + r);
      }
      const unsigned TL = pw * 16384;
#pragma unroll
      for (int c = 0; c < 4; ++c)
#pragma unroll
        for (int i = 0; i < 4; ++i)
          lwr8(TL + (lane & 31) * 272 + (32 * c + 8 * i + 4 * hf) * 2, u2{pk(O[c][4 * i], O[c][4 * i + 1]), pk(O[c][4 * i + 2], O[c][4 * i + 3])});
#pragma unroll
      for (int i = 0; i < 8; ++i) {
        const int h = 4 * i + g;
        st16(ws, OFF_OP + (h * 4 + pw) * 32768 + (b * 32 + split) * 256 + n * 16, lrd16(TL + h * 272 + n * 16));
      }
      STAMP(9);
      VMWAIT(0);
      bar();
      STAMP(10);
    }
  } else {
    const int h = xc * 4 + (idx >> 2), j = idx & 3, cu = h * 4 + j;
    const int fc = (idx >= 4 && idx < 10 && xc * 6 + idx - 4 < 43) ? xc * 6 + idx - 4 : -1;
    const int dc = idx < 4 ? xc * 4 + idx : -1;
    constexpr unsigned SCR = 147456, XN = SCR, QB = SCR + 2048, CX = SCR + 3072, RD = SCR + 4096, HL = SCR + 8192;
    const int wl = w & 3;
    u2 e = ld8(ws, OFF_E);
    const u4 S4 = {SENT, SENT, SENT, SENT};
    u4 uk[2][4];
    if (w >= 4) {
#pragma unroll
      for (int i = 0; i < 8; ++i) {
        const int row = wl * 32 + i * 4 + g;
        glds(P.wq + (size_t)(j * 128 + row) * 8192 + h * 256 + ((n * 16) ^ wsw(row)), (unsigned)((wl * 32 + i * 4) * 256));
      }
      bar();
    } else {
      u4 xr = *(const u4*)(P.x + w * 1024 + lane * 16);
      st16(ws, OFF_AP + cu * 8192 + tid * 16, S4);
      st16(ws, OFF_AP + cu * 8192 + 4096 + tid * 16, S4);
      unsigned off; bool on = true;
      if (w == 0) off = OFF_Q + g * 32768 + h * 1024 + j * 256 + n * 16;
      else if (w == 1) { off = OFF_ML + cu * 256 + lane * 16; on = lane < 16; }
      else if (w == 2) { off = OFF_H + (lane >> 2) * 8192 + cu * 64 + bb * 16; on = lane < 4 * RH; }
      else { off = OFF_HH + g * 12288 + fc * 256 + n * 16; on = fc >= 0 && g < RHH; if (RHH > 4 && on) st16(ws, off + 4 * 12288, S4); }
      if (on) st16(ws, off, S4);
      FENCE();
#pragma unroll
      for (int t = 0; t < 2; ++t)
#pragma unroll
        for (int s = 0; s < 4; ++s)
          uk[t][s] = __builtin_nontemporal_load((const u4*)(P.wuk + (size_t)(j * 128 + w * 32 + 8 * (n >> 2) + 4 * t + (n & 3)) * 8192 + h * 256 + s * 64 + g * 16));
      FENCE();
      float xv[8] = {bflo(xr.x), bfhi(xr.x), bflo(xr.y), bfhi(xr.y), bflo(xr.z), bfhi(xr.z), bflo(xr.w), bfhi(xr.w)};
      float s = 0;
#pragma unroll
      for (int i = 0; i < 8; ++i) s += xv[i];
      const float mean = sum_all(s) * (1.f / 512.f);
      float q2 = 0;
#pragma unroll
      for (int i = 0; i < 8; ++i) { xv[i] -= mean; q2 += xv[i] * xv[i]; }
      const float rs = __builtin_amdgcn_rsqf(sum_all(q2) * (1.f / 512.f) + 1e-5f);
      if (g == j) lwr16(XN + w * 256 + n * 16, u4{pk(xv[0] * rs, xv[1] * rs), pk(xv[2] * rs, xv[3] * rs), pk(xv[4] * rs, xv[5] * rs), pk(xv[6] * rs, xv[7] * rs)});
      VMWAIT(8);
      bar();
    }
    const unsigned elo = e.x + 1, ehi = e.y + (elo == 0);
    if (tid < 8) st8(ws, OFF_FLAGC + tid * 1024 + cu * 8, u2{elo, ehi});
    STAMP(1);
    if (w >= 4) VMWAIT(0);
    bar();
    STAMP(2);
    {
      f4 a = {0, 0, 0, 0};
#pragma unroll
      for (int s = 0; s < 4; ++s) {
        const int r0 = 32 * s + 8 * g + tq, r1 = r0 + 4;
        a = mfma16(trfrag(r0 * 256 + ((w * 32 + tp * 8) ^ wsw(r0)), r1 * 256 + ((w * 32 + tp * 8) ^ wsw(r1))), asb(lrd16(XN + bb * 256 + s * 64 + g * 16)), a);
      }
      if (v < 2) st16(ws, OFF_QP + cu * 4096 + (bb * 128 + w * 16 + 4 * g + 2 * v) * 8, v ? u4{asu(a[2]), elo, asu(a[3]), elo} : u4{asu(a[0]), elo, asu(a[1]), elo});
    }
    STAMP(3);
    if (w >= 4) {
      u4 t[4];
      for (int it = 0; it < SPIN_CAP; ++it) {
        FENCE();
        bool ok = true;
#pragma unroll
        for (int i = 0; i < 4; ++i) t[i] = ld16(ws, OFF_QP + (h * 4 + i) * 4096 + (tid - 256) * 16);
#pragma unroll
        for (int i = 0; i < 4; ++i) ok &= (t[i].y == elo && t[i].w == elo);
        if (__all(ok)) break;
      }
      lwr4(QB + (tid - 256) * 4, pk((asf(t[0].x) + asf(t[1].x)) + (asf(t[2].x) + asf(t[3].x)), (asf(t[0].z) + asf(t[1].z)) + (asf(t[2].z) + asf(t[3].z))));
    }
    bar();
    STAMP(4);
    if (w < 4) {
      f4 a0 = {0, 0, 0, 0}, a1 = {0, 0, 0, 0};
#pragma unroll
      for (int s = 0; s < 4; ++s) {
        bf8 qb = asb(lrd16(QB + bb * 256 + s * 64 + g * 16));
        a0 = mfma16(asb(uk[0][s]), qb, a0); a1 = mfma16(asb(uk[1][s]), qb, a1);
      }
      const float sc = 0.12751721f;
      if (v == 0) st16(ws, OFF_Q + bb * 32768 + h * 1024 + j * 256 + w * 64 + g * 16,
                       u4{nosent(pk(a0[0] * sc, a0[1] * sc)), nosent(pk(a0[2] * sc, a0[3] * sc)), nosent(pk(a1[0] * sc, a1[1] * sc)), nosent(pk(a1[2] * sc, a1[3] * sc))});
    }
    bar();
    STAMP(5);
    if (TAIL_DELAY) SLEEP(TAIL_DELAY);
    const u2 xres = *(const u2*)(P.x + bb * 1024 + cu * 8);
#pragma unroll
    for (int i = 0; i < 4; ++i) {
      const int row = w * 16 + i * 4 + g;
      glds(P.wuv + (size_t)(j * 128 + row) * 8192 + h * 256 + ((n * 16) ^ wsw(row)), (unsigned)(32768 + (w * 16 + i * 4) * 256));
    }
    if (fc >= 0) {
      const char* src = ((lane & 4) ? P.wu : P.wg) + fc * 64 + (lane & 3) * 16;
#pragma unroll
      for (int i = 0; i < 8; ++i) glds(src + (size_t)(w * 64 + i * 8 + (lane >> 3)) * 2752, (unsigned)(65536 + (w * 64 + i * 8) * 128));
    } else if (dc >= 0) {
      for (int i = w; i < 43; i += 8) glds(P.wd + (size_t)(i * 32 + (lane >> 1)) * 1024 + dc * 32 + (lane & 1) * 16, (unsigned)(65536 + i * 1024));
    }
    if (w == 0) wait_flagc(ws, lane, xc, elo, ehi);
    VMWAIT(0);
    bar();
    bf8 uv[4], ff[16];
#pragma unroll
    for (int s = 0; s < 4; ++s) {
      const int r0 = 32 * s + 8 * g + tq, r1 = r0 + 4;
      uv[s] = trfrag(32768 + r0 * 256 + ((w * 32 + tp * 8) ^ wsw(r0)), 32768 + r1 * 256 + ((w * 32 + tp * 8) ^ wsw(r1)));
    }
    if (fc >= 0) {
      const unsigned a0 = 65536 + (16 * g + tq) * 128 + (tp >> 1) * 64 + wl * 16 + (tp & 1) * 8;
#pragma unroll
      for (int k = 0; k < 16; ++k) ff[k] = trfrag(a0 + (128 * (k >> 2) + 4 * (k & 3)) * 128, a0 + (128 * (k >> 2) + 4 * (k & 3) + 64) * 128);
    } else {
#pragma unroll
      for (int k = 0; k < 16; ++k) {
        int row = 8 * (16 * (3 * wl + (k >> 2)) + 4 * g + (k & 3)) + tq; row = row > 1371 ? 1368 : row;
        ff[k] = trfrag(65536 + row * 32 + tp * 8, 65536 + row * 32 + tp * 8 + 128);
      }
    }
    bar();
#pragma unroll
    for (int i = 0; i < 16; ++i) {
      const int row = w * 16 + i;
      glds(P.wo + (size_t)(h * 128 + row) * 1024 + ((lane * 16) ^ wsw(row)), (unsigned)(row * 1024));
    }
    VMWAIT(0);
    bar();
    bf8 wo[4][4];
#pragma unroll
    for (int t = 0; t < 4; ++t)
#pragma unroll
      for (int s = 0; s < 4; ++s) {
        const int r0 = 32 * s + 8 * g + tq, r1 = r0 + 4;
        wo[t][s] = trfrag(r0 * 1024 + ((w * 128 + t * 32 + tp * 8) ^ wsw(r0)), r1 * 1024 + ((w * 128 + t * 32 + tp * 8) ^ wsw(r1)));
      }
    asm volatile("s_waitcnt lgkmcnt(0)" ::: "memory");
    STAMP(6);
    {
      u2 ml;
      for (int it = 0; it < SPIN_CAP; ++it) {
        FENCE();
        ml = ld8(ws, OFF_ML + h * 1024 + (wl * 32 + (lane & 31)) * 8);
        if (__all(ml.x != SENT && ml.y != SENT)) break;
        SLEEP(2);
      }
      FENCE();
      STAMP(7);
      u4 o[4];
#pragma unroll
      for (int i = 0; i < 4; ++i) o[i] = ld16(ws, OFF_OP + cu * 32768 + wl * 8192 + (w >> 2) * 4096 + i * 1024 + lane * 16);
      float mx = asf(ml.x);
      mx = fmaxf(mx, dpp<0xB1>(mx)); mx = fmaxf(mx, dpp<0x4E>(mx)); mx = fmaxf(mx, dpp<0x124>(mx)); mx = fmaxf(mx, dpp<0x128>(mx)); mx = max16(mx);
      const float ex = __builtin_amdgcn_exp2f(asf(ml.x) - mx);
      float L = ex * asf(ml.y);
      L += dpp<0xB1>(L); L += dpp<0x4E>(L); L = add16(sum_v(L));
      const float wt = ex * __builtin_amdgcn_rcpf(L);
      float wi[4];
#pragma unroll
      for (int i = 0; i < 4; ++i) wi[i] = asf(__builtin_amdgcn_ds_bpermute(((w >> 2) * 16 + 4 * i + g) * 4, asu(wt)));
      float acc[8] = {0, 0, 0, 0, 0, 0, 0, 0};
#pragma unroll
      for (int i = 0; i < 4; ++i) {
        acc[0] += wi[i] * bflo(o[i].x); acc[1] += wi[i] * bfhi(o[i].x); acc[2] += wi[i] * bflo(o[i].y); acc[3] += wi[i] * bfhi(o[i].y);
        acc[4] += wi[i] * bflo(o[i].z); acc[5] += wi[i] * bfhi(o[i].z); acc[6] += wi[i] * bflo(o[i].w); acc[7] += wi[i] * bfhi(o[i].w);
      }
#pragma unroll
      for (int i = 0; i < 8; ++i) acc[i] = add32(add16(acc[i]));
      if (g == 0) lwr16(XN + w * 256 + n * 16, u4{pk(acc[0], acc[1]), pk(acc[2], acc[3]), pk(acc[4], acc[5]), pk(acc[6], acc[7])});
    }
    bar();
    STAMP(8);
    {
      f4 a = {0, 0, 0, 0};
#pragma unroll
      for (int s = 0; s < 8; ++s) a = mfma16(uv[s & 3], asb(lrd16(XN + (s >> 2) * 1024 + bb * 256 + (s & 3) * 64 + g * 16)), a);
      if (v == 0) lwr8(CX + bb * 256 + (w * 16 + 4 * g) * 2, u2{pk(a[0], a[1]), pk(a[2], a[3])});
    }
    bar();
    {
      bf8 cbv[4];
#pragma unroll
      for (int s = 0; s < 4; ++s) cbv[s] = asb(lrd16(CX + bb * 256 + s * 64 + g * 16));
      f4 a[4] = {{0, 0, 0, 0}, {0, 0, 0, 0}, {0, 0, 0, 0}, {0, 0, 0, 0}};
#pragma unroll
      for (int s = 0; s < 4; ++s)
#pragma unroll
        for (int t = 0; t < 4; ++t) a[t] = mfma16(wo[t][s], cbv[s], a[t]);
      const f4 r = sel4(v, a[0], a[1], a[2], a[3]);
      st16(ws, OFF_AP + (w * 16 + v * 4 + g) * 8192 + cu * 64 + bb * 16, u4{nosent(asu(r[0])), nosent(asu(r[1])), nosent(asu(r[2])), nosent(asu(r[3]))});
    }
    STAMP(9);
    if (cu == 0 && tid == 0) st8(ws, OFF_E, u2{elo, ehi});
    if (w == 7) {
      u4 t[8];
      for (int it = 0; it < SPIN_CAP; ++it) {
        FENCE();
        bool ok = true;
#pragma unroll
        for (int i = 0; i < 8; ++i) t[i] = ld16(ws, OFF_AP + cu * 8192 + i * 1024 + lane * 16);
#pragma unroll
        for (int i = 0; i < 8; ++i) ok &= ok4(t[i]);
        if (__all(ok)) break;
      }
      f4 s = {0, 0, 0, 0};
#pragma unroll
      for (int i = 0; i < 8; ++i) s += __builtin_bit_cast(f4, t[i]);
      const float h0 = sum_vg(s[0]) + bflo(xres.x), h1 = sum_vg(s[1]) + bfhi(xres.x), h2 = sum_vg(s[2]) + bflo(xres.y), h3 = sum_vg(s[3]) + bfhi(xres.y);
      if (lane < 4 * RH) st16(ws, OFF_H + (lane >> 2) * 8192 + cu * 64 + bb * 16, u4{nosent(asu(h0)), nosent(asu(h1)), nosent(asu(h2)), nosent(asu(h3))});
    }
    if (fc >= 0 || dc >= 0) bar();
    STAMP(10);
    if (fc >= 0) {
      {
        u4 t;
        for (int it = 0; it < SPIN_CAP; ++it) {
          FENCE();
          t = ld16(ws, OFF_H + (xc % RH) * 8192 + tid * 16);
          if (__all(ok4(t))) break;
        }
        lwr16(HL + tid * 16, t);
      }
      bar();
      STAMP(11);
      if (w < 4) {
        f4 hv[8]; f4 s4v = {0, 0, 0, 0};
#pragma unroll
        for (int i = 0; i < 8; ++i) { hv[i] = __builtin_bit_cast(f4, lrd16(HL + i * 1024 + lane * 16)); s4v += hv[i]; }
        const float mean = sum_vg((s4v[0] + s4v[1]) + (s4v[2] + s4v[3])) * (1.f / 512.f);
        f4 q4 = {0, 0, 0, 0};
#pragma unroll
        for (int i = 0; i < 8; ++i) { hv[i] -= mean; q4 += hv[i] * hv[i]; }
        const float rs = __builtin_amdgcn_rsqf(sum_vg((q4[0] + q4[1]) + (q4[2] + q4[3])) * (1.f / 512.f) + 1e-5f);
        f4 a[4] = {{0, 0, 0, 0}, {0, 0, 0, 0}, {0, 0, 0, 0}, {0, 0, 0, 0}};
#pragma unroll
        for (int u = 0; u < 4; ++u) {
          const f4 y0 = hv[2 * u] * rs, y1 = hv[2 * u + 1] * rs;
          const bf8 hb = asb(u4{pk(y0[0], y0[1]), pk(y0[2], y0[3]), pk(y1[0], y1[1]), pk(y1[2], y1[3])});
#pragma unroll
          for (int vv = 0; vv < 4; ++vv) a[vv] = mfma16(ff[u * 4 + vv], hb, a[vv]);
        }
        const f4 r = sel4(v, a[0], a[1], a[2], a[3]);
        float hh[4];
#pragma unroll
        for (int i = 0; i < 4; ++i) {
          const float z = sum_v(r[i]);
          auto sw = __builtin_amdgcn_permlane32_swap(asu(z), asu(z), false, false);
          const float gt = asf(sw[0]), up = asf(sw[1]);
          const float inner = 0.7978845608f * (gt + 0.044715f * gt * gt * gt);
          hh[i] = gt * __builtin_amdgcn_rcpf(1.f + __builtin_amdgcn_exp2f(-2.885390082f * inner)) * up;
        }
        const unsigned p0 = nosent(pk(hh[0], hh[1])), p1 = nosent(pk(hh[2], hh[3]));
        auto s0 = __builtin_amdgcn_permlane16_swap(p0, p0, false, false);
        auto s1 = __builtin_amdgcn_permlane16_swap(p1, p1, false, false);
        if ((lane >> 2) < RHH) st16(ws, OFF_HH + (lane >> 2) * 12288 + (fc * 4 + w) * 64 + bb * 16, u4{s0[0], s1[0], s0[1], s1[1]});
      }
      STAMP(12);
    } else if (dc >= 0) {
      if (w < 4) {
        SLEEP(HH_DELAY);
        u4 t[3], hr;
        for (int it = 0; it < SPIN_CAP; ++it) {
          FENCE();
          hr = ld16(ws, OFF_H + (xc % RH) * 8192 + (dc * 4 + g) * 64 + bb * 16);
#pragma unroll
          for (int i = 0; i < 3; ++i) t[i] = ld16(ws, OFF_HH + (xc % RHH) * 12288 + (3 * w + i) * 1024 + lane * 16);
          bool ok = ok4(hr);
#pragma unroll
          for (int i = 0; i < 3; ++i) { const bool live = (3 * w + i) * 16 + (lane >> 2) < 172; ok &= (!live || ok4(t[i])); if (!live) t[i] = u4{0, 0, 0, 0}; }
          if (__all(ok)) break;
        }
        STAMP(13);
        f4 a[4] = {{0, 0, 0, 0}, {0, 0, 0, 0}, {0, 0, 0, 0}, {0, 0, 0, 0}};
#pragma unroll
        for (int i = 0; i < 3; ++i)
#pragma unroll
          for (int vv = 0; vv < 4; ++vv) a[vv] = mfma16(ff[i * 4 + vv], asb(t[i]), a[vv]);
        f4 r = sel4(v, a[0], a[1], a[2], a[3]);
#pragma unroll
        for (int i = 0; i < 4; ++i) r[i] = sum_v(r[i]);
        if (w == 0) r += __builtin_bit_cast(f4, hr);
        if (v == 0) lwr16(RD + w * 256 + (g * 4 + bb) * 16, __builtin_bit_cast(u4, r));
      }
      bar();
      if (w == 0 && v == 0) {
        f4 o = {0, 0, 0, 0};
#pragma unroll
        for (int i = 0; i < 4; ++i) o += __builtin_bit_cast(f4, lrd16(RD + i * 256 + (g * 4 + bb) * 16));
        *(u2*)(P.out + bb * 1024 + dc * 32 + g * 8) = u2{pk(o[0], o[1]), pk(o[2], o[3])};
      }
      STAMP(14);
    }
  }
}

extern "C" void kernel_launch(void* const* d_in, const int* in_sizes, int n_in, void* d_out, int out_size, void* d_ws, size_t ws_size,
                              hipStream_t stream) {
  Params p;
  p.x = (const char*)d_in[0]; p.ckv = (const char*)d_in[1]; p.wq = (const char*)d_in[2]; p.wuk = (const char*)d_in[3];
  p.wuv = (const char*)d_in[4]; p.wo = (const char*)d_in[5]; p.wg = (const char*)d_in[7]; p.wu = (const char*)d_in[8];
  p.wd = (const char*)d_in[9]; p.out = (char*)d_out; p.ws = (char*)d_ws;
  (void)hipFuncSetAttribute((const void*)fused, hipFuncAttributeMaxDynamicSharedMemorySize, 163840);
  fused<<<256, 512, 163840, stream>>>(p);
}
```

```cpp
#include <hip/hip_runtime.h>
#include <hip/hip_bf16.h>

typedef short s4 __attribute__((ext_vector_type(4)));
typedef __bf16 bf8 __attribute__((ext_vector_type(8)));
typedef __bf16 bf2 __attribute__((ext_vector_type(2)));
typedef float f2 __attribute__((ext_vector_type(2)));
typedef float f4 __attribute__((ext_vector_type(4)));
typedef float f16v __attribute__((ext_vector_type(16)));
typedef unsigned u2 __attribute__((ext_vector_type(2)));
typedef unsigned u4 __attribute__((ext_vector_type(4)));
#define LAS __attribute__((address_space(3)))
#define DI __device__ __forceinline__
typedef __amdgpu_buffer_rsrc_t rsrc_t;

constexpr unsigned OFF_E = 0, OFF_FLAGC = 0x1000, OFF_QP = 0x10000, OFF_Q = 0x90000, OFF_ML = 0xB0000, OFF_H = 0xC0000,
                   OFF_HH = 0xE0000, OFF_AP = 0x100000, OFF_OP = 0x200000, OFF_STAMP = 0x800000, WS_USED = 0x900000;
constexpr unsigned SENT = 0xFFFFFFFFu;
constexpr int SPIN_CAP = 200000;
constexpr int AUX_SC1 = 16, AUX_NT = 2;
#define RH 8
#define RHH 8
#define ATT_DELAY 0
#define TAIL_DELAY 0
#define HH_DELAY 24
#define FENCE() asm volatile("" ::: "memory")
#define SLEEP(n) __builtin_amdgcn_s_sleep(n)
#define ROLE_OF(b) (((b) >> 3) & 1)

#define STAMP(i)

DI void glds(const void* gsrc, unsigned lds_dst) {
  unsigned keep;
  asm volatile("s_mov_b32 %0, m0\n\ts_mov_b32 m0, %2\n\ts_nop 0\n\tglobal_load_lds_dwordx4 %1, off nt\n\ts_mov_b32 m0, %0"
               : "=&s"(keep) : "v"(gsrc), "s"(lds_dst) : "memory");
}
#define VMWAIT(n) asm volatile("s_waitcnt vmcnt(" #n ")" ::: "memory")
DI void bar() { asm volatile("s_waitcnt lgkmcnt(0)\n\ts_barrier" ::: "memory"); }
DI unsigned pk(float a, float b) { f2 x = {a, b}; bf2 y = __builtin_convertvector(x, bf2); return __builtin_bit_cast(unsigned, y); }
DI unsigned nosent(unsigned v) { return v < 0xFFFFFFFEu ? v : 0xFFFFFFFEu; }
DI float bflo(unsigned v) { return __builtin_bit_cast(float, v << 16); }
DI float bfhi(unsigned v) { return __builtin_bit_cast(float, v & 0xFFFF0000u); }
DI float asf(unsigned v) { return __builtin_bit_cast(float, v); }
DI unsigned asu(float v) { return __builtin_bit_cast(unsigned, v); }
template <int C> DI float dpp(float x) { return asf(__builtin_amdgcn_update_dpp(0u, asu(x), C, 0xF, 0xF, true)); }
DI float add16(float x) { auto s = __builtin_amdgcn_permlane16_swap(asu(x), asu(x), false, false); return asf(s[0]) + asf(s[1]); }
DI float add32(float x) { auto s = __builtin_amdgcn_permlane32_swap(asu(x), asu(x), false, false); return asf(s[0]) + asf(s[1]); }
DI float max16(float x) { auto s = __builtin_amdgcn_permlane16_swap(asu(x), asu(x), false, false); return fmaxf(asf(s[0]), asf(s[1])); }
DI float max32(float x) { auto s = __builtin_amdgcn_permlane32_swap(asu(x), asu(x), false, false); return fmaxf(asf(s[0]), asf(s[1])); }
DI float sum_v(float x) { x += dpp<0x128>(x); x += dpp<0x124>(x); return x; }
DI float sum_vg(float x) { return add32(add16(sum_v(x))); }
DI float sum_all(float x) { x += dpp<0xB1>(x); x += dpp<0x4E>(x); return sum_vg(x); }
DI u4 ld16(rsrc_t r, unsigned off) { return __builtin_amdgcn_raw_buffer_load_b128(r, off, 0, AUX_SC1); }
DI u2 ld8(rsrc_t r, unsigned off) { return __builtin_amdgcn_raw_buffer_load_b64(r, off, 0, AUX_SC1); }
DI void st16(rsrc_t r, unsigned off, u4 v) { __builtin_amdgcn_raw_buffer_store_b128(v, r, off, 0, AUX_SC1); }
DI void st8(rsrc_t r, unsigned off, u2 v) { __builtin_amdgcn_raw_buffer_store_b64(v, r, off, 0, AUX_SC1); }
DI bool ok4(u4 v) { return v.x != SENT && v.y != SENT && v.z != SENT && v.w != SENT; }
DI u4 lrd16(unsigned a) { return *(LAS u4*)a; }
DI void lwr16(unsigned a, u4 v) { *(LAS u4*)a = v; }
DI void lwr8(unsigned a, u2 v) { *(LAS u2*)a = v; }
DI void lwr4(unsigned a, unsigned v) { *(LAS unsigned*)a = v; }
DI float lrdf(unsigned a) { return *(LAS float*)a; }
DI bf8 trfrag(unsigned a0, unsigned a1) {
  s4 x = __builtin_amdgcn_ds_read_tr16_b64_v4i16((LAS s4*)a0), y = __builtin_amdgcn_ds_read_tr16_b64_v4i16((LAS s4*)a1);
  return __builtin_bit_cast(bf8, __builtin_shufflevector(x, y, 0, 1, 2, 3, 4, 5, 6, 7));
}
DI bf8 asb(u4 v) { return __builtin_bit_cast(bf8, v); }
DI f4 mfma16(bf8 a, bf8 b, f4 c) { return __builtin_amdgcn_mfma_f32_16x16x32_bf16(a, b, c, 0, 0, 0); }
DI f16v mfma32(bf8 a, bf8 b, f16v c) { return __builtin_amdgcn_mfma_f32_32x32x16_bf16(a, b, c, 0, 0, 0); }
DI f4 sel4(int v, f4 a, f4 b, f4 c, f4 d) { f4 x = (v & 1) ? b : a, y = (v & 1) ? d : c; return (v & 2) ? y : x; }
DI unsigned wsw(unsigned row) { return ((row & 3) | ((row >> 1) & 4)) << 5; }

struct Params {
  const char *x, *ckv, *wq, *wuk, *wuv, *wo, *wg, *wu, *wd;
  char *out, *ws;
};

DI void wait_flagc(rsrc_t ws, int lane, int rep, unsigned elo, unsigned ehi) {
  for (int it = 0; it < SPIN_CAP; ++it) {
    FENCE();
    u4 f = ld16(ws, OFF_FLAGC + rep * 1024 + lane * 16);
    if (__all(f.x == elo && f.y == ehi && f.z == elo && f.w == ehi)) break;
    SLEEP(8);
  }
  FENCE();
}

__global__ __launch_bounds__(512, 2) void fused(Params P) {
  const int tid = threadIdx.x, lane = tid & 63, w = __builtin_amdgcn_readfirstlane(tid >> 6);
  const int bid = blockIdx.x, xc = bid & 7, idx = bid >> 4;
  const int n = lane & 15, g = lane >> 4, bb = lane & 3, v = (lane >> 2) & 3;
  const int tq = (lane & 15) >> 2, tp = lane & 3;
  const rsrc_t ws = __builtin_amdgcn_make_buffer_rsrc(P.ws, 0, WS_USED, 0x00020000);
  STAMP(0);
  if (ROLE_OF(bid)) {
    const int b = xc >> 1, split = (xc & 1) * 16 + idx;
    const char* cb = P.ckv + ((size_t)b * 8192 + split * 256) * 1024;
    constexpr unsigned QL = 131072, PL = QL, MXL = QL + 8192, LSL = QL + 8192 + 1024;
    constexpr float TH = 40.f;
    if (w < 4) {
      u2 e = ld8(ws, OFF_E);
      if (ATT_DELAY) SLEEP(ATT_DELAY);
#pragma unroll
      for (int i = 0; i < 32; ++i) { const int row = (i >> 4) * 64 + w * 16 + (i & 15); glds(cb + (size_t)row * 1024 + ((lane ^ (i & 15)) * 16), (unsigned)(row * 1024)); }
      bar();
      STAMP(1);
      const unsigned elo = e.x + 1, ehi = e.y + (elo == 0);
      wait_flagc(ws, lane, xc, elo, ehi);
      STAMP(2);
      {
        u4 q[8];
        for (int it = 0; it < SPIN_CAP; ++it) {
          FENCE();
          bool ok = true;
#pragma unroll
          for (int i = 0; i < 8; ++i) q[i] = ld16(ws, OFF_Q + b * 32768 + (w * 8 + i) * 1024 + lane * 16);
#pragma unroll
          for (int i = 0; i < 8; ++i) ok &= ok4(q[i]);
          if (__all(ok)) break;
        }
#pragma unroll
        for (int i = 0; i < 8; ++i) lwr16(QL + (w * 8 + i) * 1024 + ((lane ^ ((w * 8 + i) & 15)) * 16), q[i]);
      }
      STAMP(3);
      bar();
      bf8 qf[2][16];
#pragma unroll
      for (int ht = 0; ht < 2; ++ht)
#pragma unroll
        for (int s = 0; s < 16; ++s) qf[ht][s] = asb(lrd16(QL + (ht * 16 + n) * 1024 + g * 256 + ((s ^ n) * 16)));
      bar();
      STAMP(4);
      float m0 = -INFINITY, m1 = -INFINITY, l0 = 0.f, l1 = 0.f;
      const int krow = 4 * (n & 3) + (n >> 2);
      unsigned qkb = (w * 16 + krow) * 1024 + g * 256 + krow * 16;
      unsigned pa = PL + ((w * 2 + (g >> 1)) * 32 + n) * 16 + 8 * (g & 1), mxa = MXL;
#pragma nounroll
      for (int r = 0; r < 4; ++r) {
        bf8 a[16];
#pragma unroll
        for (int s = 0; s < 16; ++s) a[s] = asb(lrd16(qkb ^ (s * 16)));
        __builtin_amdgcn_sched_barrier(0);
        f4 s0 = {0, 0, 0, 0}, s1 = {0, 0, 0, 0};
#pragma unroll
        for (int s = 0; s < 16; ++s) { s0 = mfma16(a[s], qf[0][s], s0); s1 = mfma16(a[s], qf[1][s], s1); }
        const float x0 = max32(max16(fmaxf(fmaxf(s0[0], s0[1]), fmaxf(s0[2], s0[3]))));
        const float x1 = max32(max16(fmaxf(fmaxf(s1[0], s1[1]), fmaxf(s1[2], s1[3]))));
        if (g < 2) lwr4(mxa + (lane * 4 + w) * 4, asu(g ? x1 : x0));
        float t0, t1;
        auto emit = [&]() {
          f4 p0, p1;
#pragma unroll
          for (int i = 0; i < 4; ++i) { p0[i] = __builtin_amdgcn_exp2f(s0[i] - m0); p1[i] = __builtin_amdgcn_exp2f(s1[i] - m1); }
          t0 = (p0[0] + p0[1]) + (p0[2] + p0[3]); t1 = (p1[0] + p1[1]) + (p1[2] + p1[3]);
          lwr8(pa, u2{pk(p0[0], p0[1]), pk(p0[2], p0[3])});
          lwr8(pa + 256, u2{pk(p1[0], p1[1]), pk(p1[2], p1[3])});
        };
        emit();
        bar();
        const u4 ma = lrd16(mxa + n * 16), mb = lrd16(mxa + (16 + n) * 16);
        const float n0 = fmaxf(fmaxf(asf(ma.x), asf(ma.y)), fmaxf(asf(ma.z), asf(ma.w)));
        const float n1 = fmaxf(fmaxf(asf(mb.x), asf(mb.y)), fmaxf(asf(mb.z), asf(mb.w)));
        if (__any(n0 > m0 + TH || n1 > m1 + TH)) {
          const float k0 = fmaxf(m0, n0), k1 = fmaxf(m1, n1);
          l0 *= __builtin_amdgcn_exp2f(m0 - k0); l1 *= __builtin_amdgcn_exp2f(m1 - k1);
          m0 = k0; m1 = k1;
          emit();
          bar();
        }
        l0 += t0; l1 += t1;
        qkb ^= 65536; pa ^= 4096; mxa ^= 512;
        STAMP(5 + r);
      }
      l0 = add32(add16(l0)); l1 = add32(add16(l1));
      if (g < 2) lwr4(LSL + (lane * 4 + w) * 4, asu(g ? l1 : l0));
      bar();
      if (w == 0 && lane < 32) {
        const u4 ls = lrd16(LSL + lane * 16);
        const float L = (asf(ls.x) + asf(ls.y)) + (asf(ls.z) + asf(ls.w));
        st8(ws, OFF_ML + lane * 1024 + (b * 32 + split) * 8, u2{nosent(asu(g ? m1 : m0)), nosent(asu(L))});
      }
      STAMP(10);
    } else {
      const int pw = w - 4, hf = lane >> 5, blk = g & 1;
      bar();
      u4 Ld[32];
#pragma unroll
      for (int i = 0; i < 32; ++i) Ld[i] = __builtin_nontemporal_load((const u4*)(cb + (size_t)(128 + 4 * i + g) * 1024 + pw * 256 + n * 16));
      STAMP(1);
      bar();
      bar();
      f16v O[4];
#pragma unroll
      for (int c = 0; c < 4; ++c)
#pragma unroll
        for (int i = 0; i < 16; ++i) O[c][i] = 0.f;
      float mr = -INFINITY;
      unsigned trb[2];
#pragma unroll
      for (int t = 0; t < 2; ++t) { const int row = 4 * tq + 2 * hf + t; trb[t] = row * 1024 + pw * 256 + (((2 * blk + (tp >> 1)) ^ row) * 16) + 8 * (tp & 1); }
      const unsigned wb = g * 1024 + pw * 256 + ((n ^ g) * 16);
#pragma unroll
      for (int r = 0; r < 4; ++r) {
        bar();
        const u4 ma = lrd16(MXL + (r & 1) * 512 + (lane & 31) * 16);
        const float n0 = fmaxf(fmaxf(asf(ma.x), asf(ma.y)), fmaxf(asf(ma.z), asf(ma.w)));
        if (__any(n0 > mr + TH)) {
          const float k0 = fmaxf(mr, n0), al = __builtin_amdgcn_exp2f(mr - k0);
          mr = k0;
          if (r > 0) {
#pragma unroll
            for (int c = 0; c < 4; ++c) O[c] *= al;
          }
          bar();
        }
        const unsigned slot = (r & 1) * 65536;
#pragma unroll
        for (int ks = 0; ks < 4; ++ks) {
          const bf8 pb = asb(lrd16(PL + (r & 1) * 4096 + (ks * 64 + lane) * 16));
          bf8 a[4];
#pragma unroll
          for (int c = 0; c < 4; ++c) a[c] = trfrag(slot + ks * 16384 + (trb[0] ^ (c * 64)), slot + ks * 16384 + (trb[1] ^ (c * 64)));
          if (r < 2) {
#pragma unroll
            for (int i = 0; i < 4; ++i) lwr16(slot + (ks * 16 + 4 * i) * 1024 + (wb ^ (i * 64)), Ld[r * 16 + ks * 4 + i]);
          }
#pragma unroll
          for (int c = 0; c < 4; ++c) O[c] = mfma32(a[c], pb, O[c]);
        }
        STAMP(5 + r);
      }
      const unsigned TL = pw * 16384;
#pragma unroll
      for (int c = 0; c < 4; ++c)
#pragma unroll
        for (int i = 0; i < 4; ++i)
          lwr8(TL + (lane & 31) * 272 + (32 * c + 8 * i + 4 * hf) * 2, u2{pk(O[c][4 * i], O[c][4 * i + 1]), pk(O[c][4 * i + 2], O[c][4 * i + 3])});
#pragma unroll
      for (int i = 0; i < 8; ++i) {
        const int h = 4 * i + g;
        st16(ws, OFF_OP + (h * 4 + pw) * 32768 + (b * 32 + split) * 256 + n * 16, lrd16(TL + h * 272 + n * 16));
      }
      STAMP(9);
      VMWAIT(0);
      bar();
      STAMP(10);
    }
  } else {
    const int h = xc * 4 + (idx >> 2), j = idx & 3, cu = h * 4 + j;
    const int fc = (idx >= 4 && idx < 10 && xc * 6 + idx - 4 < 43) ? xc * 6 + idx - 4 : -1;
    const int dc = idx < 4 ? xc * 4 + idx : -1;
    constexpr unsigned SCR = 147456, XN = SCR, QB = SCR + 2048, CX = SCR + 3072, RD = SCR + 4096, HL = SCR + 8192;
    const int wl = w & 3;
    u2 e = ld8(ws, OFF_E);
    const u4 S4 = {SENT, SENT, SENT, SENT};
    u4 uk[2][4];
    if (w >= 4) {
#pragma unroll
      for (int i = 0; i < 8; ++i) {
        const int row = wl * 32 + i * 4 + g;
        glds(P.wq + (size_t)(j * 128 + row) * 8192 + h * 256 + ((n * 16) ^ wsw(row)), (unsigned)((wl * 32 + i * 4) * 256));
      }
      bar();
    } else {
      u4 xr = *(const u4*)(P.x + w * 1024 + lane * 16);
      st16(ws, OFF_AP + cu * 8192 + tid * 16, S4);
      st16(ws, OFF_AP + cu * 8192 + 4096 + tid * 16, S4);
      unsigned off; bool on = true;
      if (w == 0) off = OFF_Q + g * 32768 + h * 1024 + j * 256 + n * 16;
      else if (w == 1) { off = OFF_ML + cu * 256 + lane * 16; on = lane < 16; }
      else if (w == 2) { off = OFF_H + (lane >> 2) * 8192 + cu * 64 + bb * 16; on = lane < 4 * RH; }
      else { off = OFF_HH + g * 12288 + fc * 256 + n * 16; on = fc >= 0 && g < RHH; if (RHH > 4 && on) st16(ws, off + 4 * 12288, S4); }
      if (on) st16(ws, off, S4);
      FENCE();
#pragma unroll
      for (int t = 0; t < 2; ++t)
#pragma unroll
        for (int s = 0; s < 4; ++s)
          uk[t][s] = __builtin_nontemporal_load((const u4*)(P.wuk + (size_t)(j * 128 + w * 32 + 8 * (n >> 2) + 4 * t + (n & 3)) * 8192 + h * 256 + s * 64 + g * 16));
      FENCE();
      float xv[8] = {bflo(xr.x), bfhi(xr.x), bflo(xr.y), bfhi(xr.y), bflo(xr.z), bfhi(xr.z), bflo(xr.w), bfhi(xr.w)};
      float s = 0;
#pragma unroll
      for (int i = 0; i < 8; ++i) s += xv[i];
      const float mean = sum_all(s) * (1.f / 512.f);
      float q2 = 0;
#pragma unroll
      for (int i = 0; i < 8; ++i) { xv[i] -= mean; q2 += xv[i] * xv[i]; }
      const float rs = __builtin_amdgcn_rsqf(sum_all(q2) * (1.f / 512.f) + 1e-5f);
      if (g == j) lwr16(XN + w * 256 + n * 16, u4{pk(xv[0] * rs, xv[1] * rs), pk(xv[2] * rs, xv[3] * rs), pk(xv[4] * rs, xv[5] * rs), pk(xv[6] * rs, xv[7] * rs)});
      VMWAIT(8);
      bar();
    }
    const unsigned elo = e.x + 1, ehi = e.y + (elo == 0);
    if (tid < 8) st8(ws, OFF_FLAGC + tid * 1024 + cu * 8, u2{elo, ehi});
    STAMP(1);
    if (w >= 4) VMWAIT(0);
    bar();
    STAMP(2);
    {
      f4 a = {0, 0, 0, 0};
#pragma unroll
      for (int s = 0; s < 4; ++s) {
        const int r0 = 32 * s + 8 * g + tq, r1 = r0 + 4;
        a = mfma16(trfrag(r0 * 256 + ((w * 32 + tp * 8) ^ wsw(r0)), r1 * 256 + ((w * 32 + tp * 8) ^ wsw(r1))), asb(lrd16(XN + bb * 256 + s * 64 + g * 16)), a);
      }
      if (v < 2) st16(ws, OFF_QP + cu * 4096 + (bb * 128 + w * 16 + 4 * g + 2 * v) * 8, v ? u4{asu(a[2]), elo, asu(a[3]), elo} : u4{asu(a[0]), elo, asu(a[1]), elo});
    }
    STAMP(3);
    if (w >= 4) {
      u4 t[4];
      for (int it = 0; it < SPIN_CAP; ++it) {
        FENCE();
        bool ok = true;
#pragma unroll
        for (int i = 0; i < 4; ++i) t[i] = ld16(ws, OFF_QP + (h * 4 + i) * 4096 + (tid - 256) * 16);
#pragma unroll
        for (int i = 0; i < 4; ++i) ok &= (t[i].y == elo && t[i].w == elo);
        if (__all(ok)) break;
      }
      lwr4(QB + (tid - 256) * 4, pk((asf(t[0].x) + asf(t[1].x)) + (asf(t[2].x) + asf(t[3].x)), (asf(t[0].z) + asf(t[1].z)) + (asf(t[2].z) + asf(t[3].z))));
    }
    bar();
    STAMP(4);
    if (w < 4) {
      f4 a0 = {0, 0, 0, 0}, a1 = {0, 0, 0, 0};
#pragma unroll
      for (int s = 0; s < 4; ++s) {
        bf8 qb = asb(lrd16(QB + bb * 256 + s * 64 + g * 16));
        a0 = mfma16(asb(uk[0][s]), qb, a0); a1 = mfma16(asb(uk[1][s]), qb, a1);
      }
      const float sc = 0.12751721f;
      if (v == 0) st16(ws, OFF_Q + bb * 32768 + h * 1024 + j * 256 + w * 64 + g * 16,
                       u4{nosent(pk(a0[0] * sc, a0[1] * sc)), nosent(pk(a0[2] * sc, a0[3] * sc)), nosent(pk(a1[0] * sc, a1[1] * sc)), nosent(pk(a1[2] * sc, a1[3] * sc))});
    }
    bar();
    STAMP(5);
    if (TAIL_DELAY) SLEEP(TAIL_DELAY);
    const u2 xres = *(const u2*)(P.x + bb * 1024 + cu * 8);
#pragma unroll
    for (int i = 0; i < 4; ++i) {
      const int row = w * 16 + i * 4 + g;
      glds(P.wuv + (size_t)(j * 128 + row) * 8192 + h * 256 + ((n * 16) ^ wsw(row)), (unsigned)(32768 + (w * 16 + i * 4) * 256));
    }
    if (fc >= 0) {
      const char* src = ((lane & 4) ? P.wu : P.wg) + fc * 64 + (lane & 3) * 16;
#pragma unroll
      for (int i = 0; i < 8; ++i) glds(src + (size_t)(w * 64 + i * 8 + (lane >> 3)) * 2752, (unsigned)(65536 + (w * 64 + i * 8) * 128));
    } else if (dc >= 0) {
      for (int i = w; i < 43; i += 8) glds(P.wd + (size_t)(i * 32 + (lane >> 1)) * 1024 + dc * 32 + (lane & 1) * 16, (unsigned)(65536 + i * 1024));
    }
    if (w == 0) wait_flagc(ws, lane, xc, elo, ehi);
    VMWAIT(0);
    bar();
    bf8 uv[4], ff[16];
#pragma unroll
    for (int s = 0; s < 4; ++s) {
      const int r0 = 32 * s + 8 * g + tq, r1 = r0 + 4;
      uv[s] = trfrag(32768 + r0 * 256 + ((w * 32 + tp * 8) ^ wsw(r0)), 32768 + r1 * 256 + ((w * 32 + tp * 8) ^ wsw(r1)));
    }
    if (fc >= 0) {
      const unsigned a0 = 65536 + (16 * g + tq) * 128 + (tp >> 1) * 64 + wl * 16 + (tp & 1) * 8;
#pragma unroll
      for (int k = 0; k < 16; ++k) ff[k] = trfrag(a0 + (128 * (k >> 2) + 4 * (k & 3)) * 128, a0 + (128 * (k >> 2) + 4 * (k & 3) + 64) * 128);
    } else {
#pragma unroll
      for (int k = 0; k < 16; ++k) {
        int row = 8 * (16 * (3 * wl + (k >> 2)) + 4 * g + (k & 3)) + tq; row = row > 1371 ? 1368 : row;
        ff[k] = trfrag(65536 + row * 32 + tp * 8, 65536 + row * 32 + tp * 8 + 128);
      }
    }
    bar();
#pragma unroll
    for (int i = 0; i < 16; ++i) {
      const int row = w * 16 + i;
      glds(P.wo + (size_t)(h * 128 + row) * 1024 + ((lane * 16) ^ wsw(row)), (unsigned)(row * 1024));
    }
    VMWAIT(0);
    bar();
    bf8 wo[4][4];
#pragma unroll
    for (int t = 0; t < 4; ++t)
#pragma unroll
      for (int s = 0; s < 4; ++s) {
        const int r0 = 32 * s + 8 * g + tq, r1 = r0 + 4;
        wo[t][s] = trfrag(r0 * 1024 + ((w * 128 + t * 32 + tp * 8) ^ wsw(r0)), r1 * 1024 + ((w * 128 + t * 32 + tp * 8) ^ wsw(r1)));
      }
    asm volatile("s_waitcnt lgkmcnt(0)" ::: "memory");
    STAMP(6);
    {
      u2 ml;
      for (int it = 0; it < SPIN_CAP; ++it) {
        FENCE();
        ml = ld8(ws, OFF_ML + h * 1024 + (wl * 32 + (lane & 31)) * 8);
        if (__all(ml.x != SENT && ml.y != SENT)) break;
        SLEEP(2);
      }
      FENCE();
      STAMP(7);
      u4 o[4];
#pragma unroll
      for (int i = 0; i < 4; ++i) o[i] = ld16(ws, OFF_OP + cu * 32768 + wl * 8192 + (w >> 2) * 4096 + i * 1024 + lane * 16);
      float mx = asf(ml.x);
      mx = fmaxf(mx, dpp<0xB1>(mx)); mx = fmaxf(mx, dpp<0x4E>(mx)); mx = fmaxf(mx, dpp<0x124>(mx)); mx = fmaxf(mx, dpp<0x128>(mx)); mx = max16(mx);
      const float ex = __builtin_amdgcn_exp2f(asf(ml.x) - mx);
      float L = ex * asf(ml.y);
      L += dpp<0xB1>(L); L += dpp<0x4E>(L); L = add16(sum_v(L));
      const float wt = ex * __builtin_amdgcn_rcpf(L);
      float wi[4];
#pragma unroll
      for (int i = 0; i < 4; ++i) wi[i] = asf(__builtin_amdgcn_ds_bpermute(((w >> 2) * 16 + 4 * i + g) * 4, asu(wt)));
      float acc[8] = {0, 0, 0, 0, 0, 0, 0, 0};
#pragma unroll
      for (int i = 0; i < 4; ++i) {
        acc[0] += wi[i] * bflo(o[i].x); acc[1] += wi[i] * bfhi(o[i].x); acc[2] += wi[i] * bflo(o[i].y); acc[3] += wi[i] * bfhi(o[i].y);
        acc[4] += wi[i] * bflo(o[i].z); acc[5] += wi[i] * bfhi(o[i].z); acc[6] += wi[i] * bflo(o[i].w); acc[7] += wi[i] * bfhi(o[i].w);
      }
#pragma unroll
      for (int i = 0; i < 8; ++i) acc[i] = add32(add16(acc[i]));
      if (g == 0) lwr16(XN + w * 256 + n * 16, u4{pk(acc[0], acc[1]), pk(acc[2], acc[3]), pk(acc[4], acc[5]), pk(acc[6], acc[7])});
    }
    bar();
    STAMP(8);
    {
      f4 a = {0, 0, 0, 0};
#pragma unroll
      for (int s = 0; s < 8; ++s) a = mfma16(uv[s & 3], asb(lrd16(XN + (s >> 2) * 1024 + bb * 256 + (s & 3) * 64 + g * 16)), a);
      if (v == 0) lwr8(CX + bb * 256 + (w * 16 + 4 * g) * 2, u2{pk(a[0], a[1]), pk(a[2], a[3])});
    }
    bar();
    {
      bf8 cbv[4];
#pragma unroll
      for (int s = 0; s < 4; ++s) cbv[s] = asb(lrd16(CX + bb * 256 + s * 64 + g * 16));
      f4 a[4] = {{0, 0, 0, 0}, {0, 0, 0, 0}, {0, 0, 0, 0}, {0, 0, 0, 0}};
#pragma unroll
      for (int s = 0; s < 4; ++s)
#pragma unroll
        for (int t = 0; t < 4; ++t) a[t] = mfma16(wo[t][s], cbv[s], a[t]);
      const f4 r = sel4(v, a[0], a[1], a[2], a[3]);
      st16(ws, OFF_AP + (w * 16 + v * 4 + g) * 8192 + cu * 64 + bb * 16, u4{nosent(asu(r[0])), nosent(asu(r[1])), nosent(asu(r[2])), nosent(asu(r[3]))});
    }
    STAMP(9);
    if (cu == 0 && tid == 0) st8(ws, OFF_E, u2{elo, ehi});
    if (w == 7) {
      u4 t[8];
      for (int it = 0; it < SPIN_CAP; ++it) {
        FENCE();
        bool ok = true;
#pragma unroll
        for (int i = 0; i < 8; ++i) t[i] = ld16(ws, OFF_AP + cu * 8192 + i * 1024 + lane * 16);
#pragma unroll
        for (int i = 0; i < 8; ++i) ok &= ok4(t[i]);
        if (__all(ok)) break;
      }
      f4 s = {0, 0, 0, 0};
#pragma unroll
      for (int i = 0; i < 8; ++i) s += __builtin_bit_cast(f4, t[i]);
      const float h0 = sum_vg(s[0]) + bflo(xres.x), h1 = sum_vg(s[1]) + bfhi(xres.x), h2 = sum_vg(s[2]) + bflo(xres.y), h3 = sum_vg(s[3]) + bfhi(xres.y);
      if (lane < 4 * RH) st16(ws, OFF_H + (lane >> 2) * 8192 + cu * 64 + bb * 16, u4{nosent(asu(h0)), nosent(asu(h1)), nosent(asu(h2)), nosent(asu(h3))});
    }
    if (fc >= 0 || dc >= 0) bar();
    STAMP(10);
    if (fc >= 0) {
      {
        u4 t;
        for (int it = 0; it < SPIN_CAP; ++it) {
          FENCE();
          t = ld16(ws, OFF_H + (xc % RH) * 8192 + tid * 16);
          if (__all(ok4(t))) break;
        }
        lwr16(HL + tid * 16, t);
      }
      bar();
      STAMP(11);
      if (w < 4) {
        f4 hv[8]; f4 s4v = {0, 0, 0, 0};
#pragma unroll
        for (int i = 0; i < 8; ++i) { hv[i] = __builtin_bit_cast(f4, lrd16(HL + i * 1024 + lane * 16)); s4v += hv[i]; }
        const float mean = sum_vg((s4v[0] + s4v[1]) + (s4v[2] + s4v[3])) * (1.f / 512.f);
        f4 q4 = {0, 0, 0, 0};
#pragma unroll
        for (int i = 0; i < 8; ++i) { hv[i] -= mean; q4 += hv[i] * hv[i]; }
        const float rs = __builtin_amdgcn_rsqf(sum_vg((q4[0] + q4[1]) + (q4[2] + q4[3])) * (1.f / 512.f) + 1e-5f);
        f4 a[4] = {{0, 0, 0, 0}, {0, 0, 0, 0}, {0, 0, 0, 0}, {0, 0, 0, 0}};
#pragma unroll
        for (int u = 0; u < 4; ++u) {
          const f4 y0 = hv[2 * u] * rs, y1 = hv[2 * u + 1] * rs;
          const bf8 hb = asb(u4{pk(y0[0], y0[1]), pk(y0[2], y0[3]), pk(y1[0], y1[1]), pk(y1[2], y1[3])});
#pragma unroll
          for (int vv = 0; vv < 4; ++vv) a[vv] = mfma16(ff[u * 4 + vv], hb, a[vv]);
        }
        const f4 r = sel4(v, a[0], a[1], a[2], a[3]);
        float hh[4];
#pragma unroll
        for (int i = 0; i < 4; ++i) {
          const float z = sum_v(r[i]);
          auto sw = __builtin_amdgcn_permlane32_swap(asu(z), asu(z), false, false);
          const float gt = asf(sw[0]), up = asf(sw[1]);
          const float inner = 0.7978845608f * (gt + 0.044715f * gt * gt * gt);
          hh[i] = gt * __builtin_amdgcn_rcpf(1.f + __builtin_amdgcn_exp2f(-2.885390082f * inner)) * up;
        }
        const unsigned p0 = nosent(pk(hh[0], hh[1])), p1 = nosent(pk(hh[2], hh[3]));
        auto s0 = __builtin_amdgcn_permlane16_swap(p0, p0, false, false);
        auto s1 = __builtin_amdgcn_permlane16_swap(p1, p1, false, false);
        if ((lane >> 2) < RHH) st16(ws, OFF_HH + (lane >> 2) * 12288 + (fc * 4 + w) * 64 + bb * 16, u4{s0[0], s1[0], s0[1], s1[1]});
      }
      STAMP(12);
    } else if (dc >= 0) {
      if (w < 4) {
        SLEEP(HH_DELAY);
        u4 t[3], hr;
        for (int it = 0; it < SPIN_CAP; ++it) {
          FENCE();
          hr = ld16(ws, OFF_H + (xc % RH) * 8192 + (dc * 4 + g) * 64 + bb * 16);
#pragma unroll
          for (int i = 0; i < 3; ++i) t[i] = ld16(ws, OFF_HH + (xc % RHH) * 12288 + (3 * w + i) * 1024 + lane * 16);
          bool ok = ok4(hr);
#pragma unroll
          for (int i = 0; i < 3; ++i) { const bool live = (3 * w + i) * 16 + (lane >> 2) < 172; ok &= (!live || ok4(t[i])); if (!live) t[i] = u4{0, 0, 0, 0}; }
          if (__all(ok)) break;
        }
        STAMP(13);
        f4 a[4] = {{0, 0, 0, 0}, {0, 0, 0, 0}, {0, 0, 0, 0}, {0, 0, 0, 0}};
#pragma unroll
        for (int i = 0; i < 3; ++i)
#pragma unroll
          for (int vv = 0; vv < 4; ++vv) a[vv] = mfma16(ff[i * 4 + vv], asb(t[i]), a[vv]);
        f4 r = sel4(v, a[0], a[1], a[2], a[3]);
#pragma unroll
        for (int i = 0; i < 4; ++i) r[i] = sum_v(r[i]);
        if (w == 0) r += __builtin_bit_cast(f4, hr);
        if (v == 0) lwr16(RD + w * 256 + (g * 4 + bb) * 16, __builtin_bit_cast(u4, r));
      }
      bar();
      if (w == 0 && v == 0) {
        f4 o = {0, 0, 0, 0};
#pragma unroll
        for (int i = 0; i < 4; ++i) o += __builtin_bit_cast(f4, lrd16(RD + i * 256 + (g * 4 + bb) * 16));
        *(u2*)(P.out + bb * 1024 + dc * 32 + g * 8) = u2{pk(o[0], o[1]), pk(o[2], o[3])};
      }
      STAMP(14);
    }
  }
}

__global__ void tstamp(unsigned long long* p) { if (threadIdx.x == 0) *p = __builtin_amdgcn_s_memrealtime(); }
__global__ void tpad(const unsigned long long* p) { unsigned long long t0 = *p; for (int i = 0; i < 100000 && __builtin_amdgcn_s_memrealtime() - t0 < 6000; ++i) __builtin_amdgcn_s_sleep(1); }
extern "C" void kernel_launch(void* const* d_in, const int* in_sizes, int n_in, void* d_out, int out_size, void* d_ws, size_t ws_size,
                              hipStream_t stream) {
  Params p;
  p.x = (const char*)d_in[0]; p.ckv = (const char*)d_in[1]; p.wq = (const char*)d_in[2]; p.wuk = (const char*)d_in[3];
  p.wuv = (const char*)d_in[4]; p.wo = (const char*)d_in[5]; p.wg = (const char*)d_in[7]; p.wu = (const char*)d_in[8];
  p.wd = (const char*)d_in[9]; p.out = (char*)d_out; p.ws = (char*)d_ws;
  (void)hipFuncSetAttribute((const void*)fused, hipFuncAttributeMaxDynamicSharedMemorySize, 163840);
  tstamp<<<1,64,0,stream>>>((unsigned long long*)((char*)d_ws + 0x880000));
  fused<<<256, 512, 163840, stream>>>(p);
  tpad<<<1,64,0,stream>>>((unsigned long long*)((char*)d_ws + 0x880000));
}
```

```cpp
#include <hip/hip_runtime.h>
#include <hip/hip_bf16.h>

typedef short s4 __attribute__((ext_vector_type(4)));
typedef __bf16 bf8 __attribute__((ext_vector_type(8)));
typedef __bf16 bf2 __attribute__((ext_vector_type(2)));
typedef float f2 __attribute__((ext_vector_type(2)));
typedef float f4 __attribute__((ext_vector_type(4)));
typedef float f16v __attribute__((ext_vector_type(16)));
typedef unsigned u2 __attribute__((ext_vector_type(2)));
typedef unsigned u4 __attribute__((ext_vector_type(4)));
#define LAS __attribute__((address_space(3)))
#define DI __device__ __forceinline__
typedef __amdgpu_buffer_rsrc_t rsrc_t;

constexpr unsigned OFF_E = 0, OFF_F2 = 0x4000, OFF_QP = 0x10000, OFF_Q = 0x700000, OFF_ML = 0xB0000, OFF_H = 0xC0000,
                   OFF_HH = 0xE0000, OFF_IN = 0x100000, IN_SZ = 0xA100, IN_AP = 0x8000, OFF_STAMP = 0x800000, WS_USED = 0x900000;
constexpr unsigned SENT = 0xFFFFFFFFu;
constexpr int SPIN_CAP = 200000;
#define AUX_ST 16
#define AUX_LD 16
constexpr int AUX_NT = 2;
#define RH 8
#define RHH 8
#define F1POS 6
#define Q_DELAY 0
#define QP_DELAY 8
#define ML_SLEEP 4
#define WO_ROT 0
#define WO_LD blds
#define F2LATE 1
#define PRE1 0
#define HOP1 0
#define O_DELAY 0
#define MLT 0
#define PFR 1
#define QPOS 16
#define QT 620
#define PACE 8
#define TAIL_DELAY 90
#define H_DELAY 64
#define HH_DELAY 100
#define SB() __builtin_amdgcn_sched_barrier(0)
#define STR2(x) #x
#define STR(x) STR2(x)
#define FENCE() asm volatile("" ::: "memory")
#define SLEEP(n) __builtin_amdgcn_s_sleep(n)
#define ROLE_OF(b) (((b) >> 3) & 1)

#define STAMP(i)

DI void glds(const void* gsrc, unsigned lds_dst) {
  unsigned keep;
  asm volatile("s_mov_b32 %0, m0\n\ts_mov_b32 m0, %2\n\ts_nop 0\n\tglobal_load_lds_dwordx4 %1, off nt\n\ts_mov_b32 m0, %0"
               : "=&s"(keep) : "v"(gsrc), "s"(lds_dst) : "memory");
}
DI void blds(u4 rs, unsigned voff, unsigned soff, unsigned lds_dst) {
  unsigned keep;
  asm volatile("s_mov_b32 %0, m0\n\ts_mov_b32 m0, %4\n\ts_nop 0\n\tbuffer_load_dwordx4 %1, %2, %3 offen nt lds\n\ts_mov_b32 m0, %0"
               : "=&s"(keep) : "v"(voff), "s"(rs), "s"(soff), "s"(lds_dst) : "memory");
}
DI void blds0(u4 rs, unsigned voff, unsigned soff, unsigned lds_dst) {
  unsigned keep;
  asm volatile("s_mov_b32 %0, m0\n\ts_mov_b32 m0, %4\n\ts_nop 0\n\tbuffer_load_dwordx4 %1, %2, %3 offen lds\n\ts_mov_b32 m0, %0"
               : "=&s"(keep) : "v"(voff), "s"(rs), "s"(soff), "s"(lds_dst) : "memory");
}
DI u4 mkrs(const char* p, unsigned n) { const unsigned long long a = (unsigned long long)p; return u4{(unsigned)a, (unsigned)(a >> 32) & 0xFFFFu, n, 0x00020000u}; }
DI unsigned umax3(unsigned a, unsigned b, unsigned c) { unsigned m = a > b ? a : b; return m > c ? m : c; }
DI unsigned umax4(unsigned m, u4 v) { return umax3(umax3(m, v.x, v.y), v.z, v.w); }
#define VMWAIT(n) asm volatile("s_waitcnt vmcnt(" STR(n) ")" ::: "memory")
DI void bar() { asm volatile("s_waitcnt lgkmcnt(0)\n\ts_barrier" ::: "memory"); }
DI unsigned pk(float a, float b) { f2 x = {a, b}; bf2 y = __builtin_convertvector(x, bf2); return __builtin_bit_cast(unsigned, y); }
DI unsigned nosent(unsigned v) { return v < 0xFFFFFFFEu ? v : 0xFFFFFFFEu; }
DI float bflo(unsigned v) { return __builtin_bit_cast(float, v << 16); }
DI float bfhi(unsigned v) { return __builtin_bit_cast(float, v & 0xFFFF0000u); }
DI float asf(unsigned v) { return __builtin_bit_cast(float, v); }
DI unsigned asu(float v) { return __builtin_bit_cast(unsigned, v); }
template <int C> DI float dpp(float x) { return asf(__builtin_amdgcn_update_dpp(0u, asu(x), C, 0xF, 0xF, true)); }
DI float add16(float x) { auto s = __builtin_amdgcn_permlane16_swap(asu(x), asu(x), false, false); return asf(s[0]) + asf(s[1]); }
DI float add32(float x) { auto s = __builtin_amdgcn_permlane32_swap(asu(x), asu(x), false, false); return asf(s[0]) + asf(s[1]); }
DI float max16(float x) { auto s = __builtin_amdgcn_permlane16_swap(asu(x), asu(x), false, false); return fmaxf(asf(s[0]), asf(s[1])); }
DI float max32(float x) { auto s = __builtin_amdgcn_permlane32_swap(asu(x), asu(x), false, false); return fmaxf(asf(s[0]), asf(s[1])); }
DI float sum_v(float x) { x += dpp<0x128>(x); x += dpp<0x124>(x); return x; }
DI float sum_vg(float x) { return add32(add16(sum_v(x))); }
DI float sum_all(float x) { x += dpp<0xB1>(x); x += dpp<0x4E>(x); return sum_vg(x); }
DI u4 ld16(rsrc_t r, unsigned off) { return __builtin_amdgcn_raw_buffer_load_b128(r, off, 0, AUX_LD); }
DI u2 ld8(rsrc_t r, unsigned off) { return __builtin_amdgcn_raw_buffer_load_b64(r, off, 0, AUX_LD); }
DI void st16(rsrc_t r, unsigned off, u4 v) { __builtin_amdgcn_raw_buffer_store_b128(v, r, off, 0, AUX_ST); }
DI void st8(rsrc_t r, unsigned off, u2 v) { __builtin_amdgcn_raw_buffer_store_b64(v, r, off, 0, AUX_ST); }
DI bool ok4(u4 v) { return v.x != SENT && v.y != SENT && v.z != SENT && v.w != SENT; }
DI u4 lrd16(unsigned a) { return *(LAS u4*)a; }
DI void lwr16(unsigned a, u4 v) { *(LAS u4*)a = v; }
DI void lwr8(unsigned a, u2 v) { *(LAS u2*)a = v; }
DI void lwr4(unsigned a, unsigned v) { *(LAS unsigned*)a = v; }
DI float lrdf(unsigned a) { return *(LAS float*)a; }
DI bf8 trfrag(unsigned a0, unsigned a1) {
  s4 x = __builtin_amdgcn_ds_read_tr16_b64_v4i16((LAS s4*)a0), y = __builtin_amdgcn_ds_read_tr16_b64_v4i16((LAS s4*)a1);
  return __builtin_bit_cast(bf8, __builtin_shufflevector(x, y, 0, 1, 2, 3, 4, 5, 6, 7));
}
DI bf8 asb(u4 v) { return __builtin_bit_cast(bf8, v); }
DI f4 mfma16(bf8 a, bf8 b, f4 c) { return __builtin_amdgcn_mfma_f32_16x16x32_bf16(a, b, c, 0, 0, 0); }
DI f16v mfma32(bf8 a, bf8 b, f16v c) { return __builtin_amdgcn_mfma_f32_32x32x16_bf16(a, b, c, 0, 0, 0); }
DI f4 sel4(int v, f4 a, f4 b, f4 c, f4 d) { f4 x = (v & 1) ? b : a, y = (v & 1) ? d : c; return (v & 2) ? y : x; }
DI unsigned wsw(unsigned row) { return ((row & 3) | ((row >> 1) & 4)) << 5; }

struct Params {
  const char *x, *ckv, *wq, *wuk, *wuv, *wo, *wg, *wu, *wd;
  char *out, *ws;
};

#define EPOCH() const u2 e_ = *(LAS u2*)(131072 + (att ? w : 3) * 8192); const unsigned elo = __builtin_amdgcn_readfirstlane(e_.x) + 1, \
  ehi = __builtin_amdgcn_readfirstlane(e_.y) + (elo == 0)
DI bool flag_ok(u4 f, unsigned elo, unsigned ehi) { return __all(f.x == elo && f.y == ehi && f.z == elo && f.w == ehi); }

__global__ __launch_bounds__(512, 2) void fused(Params P) {
  const int tid = threadIdx.x, lane = tid & 63, w = __builtin_amdgcn_readfirstlane(tid >> 6);
  const int bid = blockIdx.x, xc = bid & 7, idx = bid >> 4;
  const int n = lane & 15, g = lane >> 4;
  const bool att = ROLE_OF(bid);
  STAMP(0);
  const unsigned t00 = (unsigned)__builtin_amdgcn_s_memrealtime();
  const rsrc_t ws = __builtin_amdgcn_make_buffer_rsrc(P.ws, 0, WS_USED, 0x00020000);
  const int h = xc * 4 + (idx >> 2), j = idx & 3, cu = xc * 16 + idx;
  const char* cb = P.ckv + (size_t)cu * 262144;
  const u4 S4 = {SENT, SENT, SENT, SENT};
  if (att ? w < 4 : w == 3) blds0(mkrs(P.ws, 16), lane * 16, 0, 131072 + w * 8192);
  if (!att && w >= 4) blds0(mkrs(P.x, 4096), lane * 16, (w & 3) * 1024, 69632 + w * 1024);
  {
    const u4 rs = mkrs(att ? cb + w * 16384 : P.wq + (size_t)(j * 128 + w * 16) * 8192 + h * 256, 0x20000);
    const unsigned v0 = att ? lane * 16 : g * 8192 + ((n * 16) ^ (g << 5));
    const unsigned xm = att ? 15 : 2, xs = att ? 4 : 6, ss = att ? 1024 : 32768, cnt = att ? 16 : 4;
    unsigned ld = w * (att ? 16384 : 4096), so = 0;
#pragma nounroll
    for (unsigned i = 0; i < cnt; ++i) { blds(rs, v0 ^ ((i & xm) << xs), so, ld); so += ss; ld += 1024; }
  }
  const int bb = lane & 3, v = (lane >> 2) & 3, tq = (lane & 15) >> 2, tp = lane & 3, wl = w & 3;
  if (!att) {
    constexpr unsigned XN = 65536, QB = XN + 2048, CT = 0, CX = 2048;
    if (w < 4) {
      const u4 rs = mkrs(P.wuk + (size_t)(j * 128 + w * 32) * 8192 + h * 256, 0x40000);
      const unsigned v0 = g * 8192 + ((n ^ g) * 16);
#pragma unroll
      for (int i = 0; i < 8; ++i) blds(rs, v0 ^ ((i & 6) << 5), i * 32768, 32768 + w * 8192 + i * 1024);
      VMWAIT(8);
    } else {
      VMWAIT(4);
      const u4 xr = lrd16(69632 + w * 1024 + lane * 16);
      STAMP(11);
      float xv[8] = {bflo(xr.x), bfhi(xr.x), bflo(xr.y), bfhi(xr.y), bflo(xr.z), bfhi(xr.z), bflo(xr.w), bfhi(xr.w)};
      float s = 0;
#pragma unroll
      for (int i = 0; i < 8; ++i) s += xv[i];
      const float mean = sum_all(s) * (1.f / 512.f);
      float q2 = 0;
#pragma unroll
      for (int i = 0; i < 8; ++i) { xv[i] -= mean; q2 += xv[i] * xv[i]; }
      const float rs = __builtin_amdgcn_rsqf(sum_all(q2) * (1.f / 512.f) + 1e-5f);
      if (g == j) lwr16(XN + wl * 320 + n * 16, u4{pk(xv[0] * rs, xv[1] * rs), pk(xv[2] * rs, xv[3] * rs), pk(xv[4] * rs, xv[5] * rs), pk(xv[6] * rs, xv[7] * rs)});
      STAMP(12);
      VMWAIT(0);
    }
    STAMP(1);
    bar();
    STAMP(2);
    EPOCH();
    {
      bf8 wa[4], xb[4];
#pragma unroll
      for (int s = 0; s < 4; ++s) {
        const int r0 = 32 * s + 8 * g + tq, r1 = r0 + 4;
        wa[s] = trfrag(r0 * 256 + ((w * 32 + tp * 8) ^ wsw(r0)), r1 * 256 + ((w * 32 + tp * 8) ^ wsw(r1)));
        xb[s] = asb(lrd16(XN + bb * 320 + s * 64 + g * 16));
      }
      SB();
      f4 a = {0, 0, 0, 0};
#pragma unroll
      for (int s = 0; s < 4; ++s) a = mfma16(wa[s], xb[s], a);
      if (v < 2) st16(ws, OFF_QP + cu * 4096 + (bb * 128 + w * 16 + 4 * g + 2 * v) * 8, v ? u4{asu(a[2]), elo, asu(a[3]), elo} : u4{asu(a[0]), elo, asu(a[1]), elo});
    }
    STAMP(3);
    bf8 uk[2][4];
    if (w >= 4) {
      u4 t[4];
      SLEEP(QP_DELAY);
#pragma nounroll
      for (int it = 0; it < SPIN_CAP; ++it) {
        FENCE();
        bool ok = true;
#pragma unroll
        for (int i = 0; i < 4; ++i) t[i] = ld16(ws, OFF_QP + (h * 4 + i) * 4096 + (tid - 256) * 16);
#pragma unroll
        for (int i = 0; i < 4; ++i) ok &= (t[i].y == elo && t[i].w == elo);
        if (__all(ok)) break;
        SLEEP(2);
      }
      lwr4(QB + wl * 320 + lane * 4, pk((asf(t[0].x) + asf(t[1].x)) + (asf(t[2].x) + asf(t[3].x)), (asf(t[0].z) + asf(t[1].z)) + (asf(t[2].z) + asf(t[3].z))));
    } else {
      VMWAIT(1);
#pragma unroll
      for (int t = 0; t < 2; ++t)
#pragma unroll
        for (int s = 0; s < 4; ++s) uk[t][s] = asb(lrd16(32768 + w * 8192 + (8 * (n >> 2) + 4 * t + (n & 3)) * 256 + (((4 * s + g) ^ n) * 16)));
    }
    bar();
    STAMP(4);
    if (w < 4) {
      bf8 qb[4];
#pragma unroll
      for (int s = 0; s < 4; ++s) qb[s] = asb(lrd16(QB + bb * 320 + s * 64 + g * 16));
      SB();
      f4 a0 = {0, 0, 0, 0}, a1 = {0, 0, 0, 0};
#pragma unroll
      for (int s = 0; s < 4; ++s) { a0 = mfma16(uk[0][s], qb[s], a0); a1 = mfma16(uk[1][s], qb[s], a1); }
      const float sc = 0.12751721f;
      const unsigned d3 = pk(a1[2] * sc, a1[3] * sc), ch = j * 16 + w * 4 + g, qo = OFF_Q + bb * 49152 + (h >> 3) * 12288;
      auto sw = __builtin_amdgcn_permlane16_swap(d3, d3, false, false);
      if (v == 0 || (v == 1 && !(g & 1)))
        st16(ws, v ? qo + 8192 + (h & 7) * 512 + ch * 8 : qo + (h & 7) * 1024 + ch * 16,
             v ? u4{sw[0], sw[1], ehi, elo} : u4{nosent(pk(a0[0] * sc, a0[1] * sc)), pk(a0[2] * sc, a0[3] * sc), pk(a1[0] * sc, a1[1] * sc), elo});
    }
    bar();
    STAMP(5);
    if (TAIL_DELAY) SLEEP(TAIL_DELAY);
    const unsigned inb = OFF_IN + cu * IN_SZ;
    const u2 xres = *(const u2*)(P.x + bb * 1024 + cu * 8);
#pragma unroll
    for (int i = 0; i < 5; ++i) st16(ws, inb + i * 8192 + tid * 16, S4);
    {
      unsigned off = WS_USED;
      if (w == 1 && lane < 4 * RH) off = OFF_H + (lane >> 2) * 8192 + cu * 64 + bb * 16;
      if (w == 2 && lane < 6 * RHH) off = OFF_HH + cu * 96 * RHH + lane * 16;
      st16(ws, off, S4);
    }
    FENCE();
    {
      const u4 ru = mkrs(P.wuv + (size_t)(j * 128 + w * 16) * 8192 + h * 256, 0x20000);
      const unsigned v0 = g * 8192 + ((n * 16) ^ (g << 5));
#pragma unroll
      for (int i = 0; i < 4; ++i) blds(ru, v0 ^ ((i & 2) << 6), i * 32768, 131072 + w * 4096 + i * 1024);
      const u4 ro = mkrs(P.wo + (size_t)h * 131072, 0x20000);
#pragma unroll
      for (int i = 0; i < 16; ++i) {
        const int row = ((i + WO_ROT * j) & 15) * 8 + w;
        WO_LD(ro, (lane * 16) ^ wsw(row), row * 1024, row * 1024);
        if (i == 3) {
          VMWAIT(8);
          bar();
          STAMP(13);
          if (tid < 8) st8(ws, OFF_F2 + tid * 1024 + cu * 8, u2{elo, ehi});
        }
      }
    }
    VMWAIT(0);
    bar();
    bf8 uv[4], wo[4][4];
#pragma unroll
    for (int s = 0; s < 4; ++s) {
      const int r0 = 32 * tq + 8 * s + 2 * g + (h & 1), r1 = r0 ^ 1;
      uv[s] = trfrag(131072 + r0 * 256 + ((w * 32 + tp * 8) ^ wsw(r0)), 131072 + r1 * 256 + ((w * 32 + tp * 8) ^ wsw(r1)));
    }
#pragma unroll
    for (int t = 0; t < 4; ++t)
#pragma unroll
      for (int s = 0; s < 4; ++s) {
        const int r0 = 32 * s + 8 * g + tq, r1 = r0 + 4;
        wo[t][s] = trfrag(r0 * 1024 + ((w * 128 + t * 32 + tp * 8) ^ wsw(r0)), r1 * 1024 + ((w * 128 + t * 32 + tp * 8) ^ wsw(r1)));
      }
    bar();
    STAMP(6);
    {
      u4 ml, o[4] = {S4, S4, S4, S4};
      const unsigned oa = inb + wl * 8192 + (lane >> 3) * 256 + (w >> 2) * 128 + (lane & 7) * 16;
      auto oload = [&]() {
#pragma unroll
        for (int i = 0; i < 4; ++i) if (umax4(0, o[i]) == SENT) o[i] = ld16(ws, oa + i * 2048);
      };
#pragma nounroll
      for (int it = 0; it < 64 && (unsigned)__builtin_amdgcn_s_memrealtime() - t00 < MLT; ++it) SLEEP(4);
#pragma nounroll
      for (int it = 0; it < SPIN_CAP; ++it) {
        FENCE();
        ml = ld16(ws, OFF_ML + h * 2048 + (wl * 32 + (lane & 31)) * 16);
        if (HOP1) oload();
        if (__all(ml.y == elo && ml.w == elo)) break;
        SLEEP(ML_SLEEP);
      }
      FENCE();
      STAMP(7);
      if (O_DELAY) SLEEP(O_DELAY);
      oload();
      float mx = asf(ml.x);
      mx = fmaxf(mx, dpp<0xB1>(mx)); mx = fmaxf(mx, dpp<0x4E>(mx)); mx = fmaxf(mx, dpp<0x124>(mx)); mx = fmaxf(mx, dpp<0x128>(mx)); mx = max16(mx);
      const float ex = __builtin_amdgcn_exp2f(asf(ml.x) - mx);
      float L = ex * asf(ml.z);
      L += dpp<0xB1>(L); L += dpp<0x4E>(L); L = add16(sum_v(L));
      const float wt = ex * __builtin_amdgcn_rcpf(L);
      float wi[4];
#pragma unroll
      for (int i = 0; i < 4; ++i) wi[i] = asf(__builtin_amdgcn_ds_bpermute((8 * i + (lane >> 3)) * 4, asu(wt)));
      for (int it = 0; it < SPIN_CAP; ++it) {
        if (__all(umax4(umax4(umax4(umax4(0, o[0]), o[1]), o[2]), o[3]) != SENT)) break;
        FENCE();
        oload();
      }
      float acc[8] = {0, 0, 0, 0, 0, 0, 0, 0};
#pragma unroll
      for (int i = 0; i < 4; ++i) {
        acc[0] += wi[i] * bflo(o[i].x); acc[1] += wi[i] * bfhi(o[i].x); acc[2] += wi[i] * bflo(o[i].y); acc[3] += wi[i] * bfhi(o[i].y);
        acc[4] += wi[i] * bflo(o[i].z); acc[5] += wi[i] * bfhi(o[i].z); acc[6] += wi[i] * bflo(o[i].w); acc[7] += wi[i] * bfhi(o[i].w);
      }
      float t[4], u[2];
#pragma unroll
      for (int i = 0; i < 4; ++i) { auto s = __builtin_amdgcn_permlane32_swap(asu(acc[i]), asu(acc[i + 4]), false, false); t[i] = asf(s[0]) + asf(s[1]); }
#pragma unroll
      for (int i = 0; i < 2; ++i) { auto s = __builtin_amdgcn_permlane16_swap(asu(t[i]), asu(t[i + 2]), false, false); u[i] = asf(s[0]) + asf(s[1]); u[i] += dpp<0x128>(u[i]); }
      lwr4(CT + wl * 320 + (w >> 2) * 128 + (lane & 7) * 16 + g * 4, pk(u[0], u[1]));
    }
    bar();
    STAMP(8);
    {
      bf8 cb4[4];
#pragma unroll
      for (int s = 0; s < 4; ++s) cb4[s] = asb(lrd16(CT + bb * 320 + s * 64 + g * 16));
      SB();
      f4 a = {0, 0, 0, 0};
#pragma unroll
      for (int s = 0; s < 4; ++s) a = mfma16(uv[s], cb4[s], a);
      lwr8(CX + bb * 320 + (w * 16 + 4 * g) * 2, u2{pk(a[0], a[1]), pk(a[2], a[3])});
    }
    bar();
    {
      bf8 cbv[4];
#pragma unroll
      for (int s = 0; s < 4; ++s) cbv[s] = asb(lrd16(CX + bb * 320 + s * 64 + g * 16));
      SB();
      f4 a[4] = {{0, 0, 0, 0}, {0, 0, 0, 0}, {0, 0, 0, 0}, {0, 0, 0, 0}};
#pragma unroll
      for (int s = 0; s < 4; ++s)
#pragma unroll
        for (int t = 0; t < 4; ++t) a[t] = mfma16(wo[t][s], cbv[s], a[t]);
      const f4 r = sel4(v, a[0], a[1], a[2], a[3]);
      st16(ws, OFF_IN + IN_AP + (w * 16 + v * 4 + g) * IN_SZ + cu * 64 + bb * 16, u4{nosent(asu(r[0])), nosent(asu(r[1])), nosent(asu(r[2])), nosent(asu(r[3]))});
    }
    STAMP(9);
    if (cu == 0 && tid == 0) st8(ws, OFF_E, u2{elo, ehi});
    if (w == 7) {
      u4 t[8];
      for (int it = 0; it < SPIN_CAP; ++it) {
        FENCE();
        unsigned m = 0;
#pragma unroll
        for (int i = 0; i < 8; ++i) t[i] = ld16(ws, inb + IN_AP + i * 1024 + lane * 16);
#pragma unroll
        for (int i = 0; i < 8; ++i) m = umax4(m, t[i]);
        if (__all(m != SENT)) break;
      }
      f4 s = {0, 0, 0, 0};
#pragma unroll
      for (int i = 0; i < 8; ++i) s += __builtin_bit_cast(f4, t[i]);
      const float h0 = sum_vg(s[0]) + bflo(xres.x), h1 = sum_vg(s[1]) + bfhi(xres.x), h2 = sum_vg(s[2]) + bflo(xres.y), h3 = sum_vg(s[3]) + bfhi(xres.y);
      if (lane < 4 * RH) st16(ws, OFF_H + (lane >> 2) * 8192 + cu * 64 + bb * 16, u4{nosent(asu(h0)), nosent(asu(h1)), nosent(asu(h2)), nosent(asu(h3))});
    }
    STAMP(10);
  } else {
    const int b = xc >> 1;
    constexpr unsigned QL = 131072, PL = QL, MXL = QL + 8192, LSL = QL + 8192 + 1024;
    constexpr float TH = 40.f;
    if (w < 4) {
      lwr4(QL + (w * 8 + 7) * 1024 + lane * 16, SENT);
      bar();
      STAMP(1);
      __builtin_amdgcn_s_setprio(3);
      VMWAIT(QPOS);
      EPOCH();
      STAMP(2);
      const int krow = 4 * (n & 3) + (n >> 2);
      unsigned qkb = (w * 16 + krow) * 1024 + g * 256 + krow * 16;
      bf8 a[16];
      {
#pragma nounroll
        for (int it = 0; it < 64 && (unsigned)__builtin_amdgcn_s_memrealtime() - t00 < QT; ++it) SLEEP(4);
        u4 q[12];
        const unsigned qa = OFF_Q + b * 49152 + w * 12288 + lane * 16;
#pragma unroll
        for (int i = 0; i < 12; ++i) q[i] = ld16(ws, qa + i * 1024);
        VMWAIT(12);
#pragma unroll
        for (int s = 0; s < 16; ++s) a[s] = asb(lrd16(qkb ^ (s * 16)));
#pragma nounroll
        for (int it = 0; it < SPIN_CAP; ++it) {
          unsigned bad = 0;
#pragma unroll
          for (int i = 0; i < 12; ++i) bad |= __any(q[i].w != elo || (i >= 8 && q[i].z != ehi)) << i;
          if (!bad) break;
          FENCE();
#pragma unroll
          for (int i = 0; i < 12; ++i) if (bad >> i & 1) q[i] = ld16(ws, qa + i * 1024);
        }
#pragma unroll
        for (int i = 0; i < 4; ++i) {
          const unsigned row = w * 8 + i * 2 + (lane >> 5), xa = QL + row * 1024 + ((((lane & 31) * 2) ^ (row & 15)) * 16) + 12;
          lwr4(xa, q[8 + i].x); lwr4(xa ^ 16, q[8 + i].y);
        }
#pragma unroll
        for (int i = 0; i < 8; ++i) {
          const unsigned ma = QL + (w * 8 + i) * 1024 + ((lane ^ ((w * 8 + i) & 15)) * 16);
          lwr4(ma + 8, q[i].z); FENCE(); lwr8(ma, u2{q[i].x, q[i].y}); FENCE();
        }
      }
      STAMP(3);
      __builtin_amdgcn_s_setprio(0);
#pragma nounroll
      for (int it = 0; it < SPIN_CAP; ++it) {
        FENCE();
        unsigned m = 0;
#pragma unroll
        for (int i = 0; i < 4; ++i) { const unsigned x = *(LAS unsigned*)(QL + (i * 8 + 7) * 1024 + lane * 16); m = x > m ? x : m; }
        if (__all(m != SENT)) break;
      }
      FENCE();
      u4 f2;
      unsigned pf0, pf1;
      bf8 qf[2][16];
#pragma unroll
      for (int s = 0; s < 16; ++s)
#pragma unroll
        for (int ht = 0; ht < 2; ++ht) qf[ht][s] = asb(lrd16(QL + (ht * 16 + n) * 1024 + g * 256 + ((s ^ n) * 16)));
      SB();
      float m0 = 0.f, m1 = 0.f, l0 = 0.f, l1 = 0.f;
      unsigned pa = PL + ((w * 2 + (g >> 1)) * 32 + n) * 16 + 8 * (g & 1), mxa = MXL;
      const unsigned lsa = LSL + n * 64 + (w * 4 + g) * 4;
#pragma unroll
      for (int r = 0; r < 4; ++r) {
        f4 s0 = {0, 0, 0, 0}, s1 = {0, 0, 0, 0};
#pragma unroll
        for (int s = 0; s < 16; ++s) { s0 = mfma16(a[s], qf[0][s], s0); s1 = mfma16(a[s], qf[1][s], s1); }
        SB();
        if (r == 0) {
          bar();
        }
        float t0, t1;
        auto emit = [&]() {
          f4 p0, p1;
#pragma unroll
          for (int i = 0; i < 4; ++i) { p0[i] = __builtin_amdgcn_exp2f(s0[i] - m0); p1[i] = __builtin_amdgcn_exp2f(s1[i] - m1); }
          lwr8(pa, u2{pk(p0[0], p0[1]), pk(p0[2], p0[3])});
          lwr8(pa + 256, u2{pk(p1[0], p1[1]), pk(p1[2], p1[3])});
          t0 = l0 + ((p0[0] + p0[1]) + (p0[2] + p0[3])); t1 = l1 + ((p1[0] + p1[1]) + (p1[2] + p1[3]));
          if (r == 3) { lwr4(lsa, asu(t0)); lwr4(lsa + 1024, asu(t1)); }
        };
        emit();
        {
          const float x0 = fmaxf(fmaxf(s0[0], s0[1]), fmaxf(s0[2], s0[3])), x1 = fmaxf(fmaxf(s1[0], s1[1]), fmaxf(s1[2], s1[3]));
          auto sw = __builtin_amdgcn_permlane16_swap(asu(x0), asu(x1), false, false);
          const float x = max32(fmaxf(asf(sw[0]), asf(sw[1])));
          lwr4(mxa + ((lane & 31) * 4 + w) * 4, asu(x));
        }
        if (r == 3 && w == 0) {
#pragma nounroll
          for (int it = 0; it < SPIN_CAP && !flag_ok(f2, elo, ehi); ++it) { FENCE(); f2 = ld16(ws, OFF_F2 + xc * 1024 + lane * 16); }
        }
        bar();
        if (r == PFR) {
          const rsrc_t rf = __builtin_amdgcn_make_buffer_rsrc((void*)(w == 1 ? P.wg : w == 2 ? P.wu : P.wd), 0, w ? 1409024 : 0, 0x00020000);
          const unsigned po = w < 3 ? (idx * 32 + (lane >> 2)) * 2752 + xc * 384 + (bb < 3 ? bb * 128 : 380) : (idx * 86 + lane) * 1024 + xc * 128;
          pf0 = __builtin_amdgcn_raw_buffer_load_b32(rf, po, 0, 0);
          pf1 = __builtin_amdgcn_raw_buffer_load_b32(rf, w < 3 ? po + 16 * 2752 : lane < 22 ? po + 65536 : 1409024, 0, 0);
        }
        if (r == 1) f2 = ld16(ws, w == 0 ? OFF_F2 + xc * 1024 + lane * 16 : WS_USED);
        if (r == 2) __builtin_amdgcn_s_setprio(3);
        const u4 ma = lrd16(mxa + n * 16), mb = lrd16(mxa + (16 + n) * 16);
        if (PRE1 ? (r == 1 || r == 2) : r < 3) {
          qkb ^= 65536;
          asm volatile("" : "+v"(qkb));
#pragma unroll
          for (int s = 0; s < 16; ++s) a[s] = asb(lrd16(qkb ^ (s * 16)));
        }
        SB();
        const float n0 = fmaxf(fmaxf(asf(ma.x), asf(ma.y)), fmaxf(asf(ma.z), asf(ma.w)));
        const float n1 = fmaxf(fmaxf(asf(mb.x), asf(mb.y)), fmaxf(asf(mb.z), asf(mb.w)));
        if (__builtin_expect(__any(r == 0 ? !(fabsf(n0) <= TH && fabsf(n1) <= TH) : (n0 > m0 + TH || n1 > m1 + TH)), 0)) {
          const float k0 = r == 0 ? n0 : fmaxf(m0, n0), k1 = r == 0 ? n1 : fmaxf(m1, n1);
          if (r > 0) { l0 *= __builtin_amdgcn_exp2f(m0 - k0); l1 *= __builtin_amdgcn_exp2f(m1 - k1); }
          m0 = k0; m1 = k1;
          bar();
          emit();
          bar();
        }
        l0 = t0; l1 = t1;
        pa ^= 4096; mxa ^= 512;
        if (r == 0 || r == 3) STAMP(5 + r);
      }
      if (w == 0) {
        f4 s = {0, 0, 0, 0};
#pragma unroll
        for (int i = 0; i < 4; ++i) s += __builtin_bit_cast(f4, lrd16(LSL + (lane & 31) * 64 + i * 16));
        if (lane < 32) st16(ws, OFF_ML + lane * 2048 + cu * 16, u4{asu(g ? m1 : m0), elo, asu((s[0] + s[1]) + (s[2] + s[3])), elo});
      } else asm volatile("" :: "v"(pf0), "v"(pf1));
      __builtin_amdgcn_s_setprio(0);
    } else {
      const int hf = lane >> 5, blk = g & 1;
      u4 Ld[32];
      const rsrc_t cr = __builtin_amdgcn_make_buffer_rsrc((void*)cb, 0, 262144, 0x00020000);
      const unsigned lp = (128 + g) * 1024 + wl * 256 + n * 16;
      bar();
#pragma unroll
      for (int i = 0; i < 32; ++i) { VMWAIT(PACE); Ld[i] = __builtin_amdgcn_raw_buffer_load_b128(cr, lp, i * 4096, AUX_NT); }
      VMWAIT(32);
      STAMP(1);
      f16v O[4];
#pragma unroll
      for (int c = 0; c < 4; ++c)
#pragma unroll
        for (int i = 0; i < 16; ++i) O[c][i] = 0.f;
      float mr = 0.f;
      unsigned trb[2];
#pragma unroll
      for (int t = 0; t < 2; ++t) { const int row = 4 * tq + 2 * hf + t; trb[t] = row * 1024 + wl * 256 + (((2 * blk + (tp >> 1)) ^ row) * 16) + 8 * (tp & 1); }
      const unsigned wb = g * 1024 + wl * 256 + ((n ^ g) * 16);
      bf8 a[4][4];
      auto rd = [&](int r, int ks) {
#pragma unroll
        for (int c = 0; c < 4; ++c) a[ks][c] = trfrag((r & 1) * 65536 + ks * 16384 + (trb[0] ^ (c * 64)), (r & 1) * 65536 + ks * 16384 + (trb[1] ^ (c * 64)));
      };
      bar();
      rd(0, 0);
#pragma unroll
      for (int r = 0; r < 4; ++r) {
        asm volatile("s_waitcnt lgkmcnt(8)\n\ts_barrier" ::: "memory");
        const u4 ma = lrd16(MXL + (r & 1) * 512 + (lane & 31) * 16);
        bf8 pb[4];
#pragma unroll
        for (int ks = 0; ks < (r ? 4 : 1); ++ks) pb[ks] = asb(lrd16(PL + (r & 1) * 4096 + (ks * 64 + lane) * 16));
        SB();
        const float n0 = fmaxf(fmaxf(asf(ma.x), asf(ma.y)), fmaxf(asf(ma.z), asf(ma.w)));
        if (__builtin_expect(__any(r == 0 ? !(fabsf(n0) <= TH) : n0 > mr + TH), 0)) {
          const float k0 = r == 0 ? n0 : fmaxf(mr, n0), al = __builtin_amdgcn_exp2f(mr - k0);
          mr = k0;
          bar();
          if (r > 0) {
#pragma unroll
            for (int i = 0; i < 16; ++i) {
              const float z = asf(__builtin_amdgcn_ds_bpermute((8 * (i >> 2) + 4 * hf + (i & 3)) * 4, asu(al)));
#pragma unroll
              for (int c = 0; c < 4; ++c) O[c][i] *= z;
            }
          }
          bar();
#pragma unroll
          for (int ks = 0; ks < (r ? 4 : 1); ++ks) pb[ks] = asb(lrd16(PL + (r & 1) * 4096 + (ks * 64 + lane) * 16));
        }
#pragma unroll
        for (int ks = 0; ks < 4; ++ks) {
          if (r < 2) {
            if (ks < 3) { rd(r, ks + 1); if (r == 0) pb[ks + 1] = asb(lrd16(PL + (ks * 64 + 64 + lane) * 16)); }
          }
          SB();
#pragma unroll
          for (int c = 0; c < 4; ++c) {
            O[c] = mfma32(pb[ks], a[ks][c], O[c]);
            if (r < 2) { SB(); lwr16((r & 1) * 65536 + (ks * 16 + 4 * c) * 1024 + (wb ^ (c * 64)), Ld[r * 16 + ks * 4 + c]); SB(); }
          }
          SB();
          if (r == 0 && ks == 3) rd(1, 0);
          if (r == 1 || r == 2) rd(r + 1, ks);
          SB();
        }
        if (r == 0 || r == 3) STAMP(5 + r);
      }
      const bool odd = lane & 1;
      const unsigned ob = OFF_IN + (hf * 16 + (lane & 1) * 4 + wl) * IN_SZ + cu * 256 + (lane & 30) * 8;
#pragma unroll
      for (int i = 0; i < 16; i += 2) {
        const unsigned a0 = nosent(pk(O[0][i], O[1][i])), a1 = nosent(pk(O[2][i], O[3][i]));
        const unsigned b0 = nosent(pk(O[0][i + 1], O[1][i + 1])), b1 = nosent(pk(O[2][i + 1], O[3][i + 1]));
        const unsigned y0 = __builtin_amdgcn_update_dpp(0u, odd ? a0 : b0, 0xB1, 0xF, 0xF, true), y1 = __builtin_amdgcn_update_dpp(0u, odd ? a1 : b1, 0xB1, 0xF, 0xF, true);
        __builtin_amdgcn_raw_buffer_store_b128(u4{odd ? b0 : a0, odd ? b1 : a1, y0, y1}, ws, ob, (8 * (i >> 2) + (i & 3)) * 4 * IN_SZ, AUX_ST);
      }
    }
    STAMP(9);
    const int fc = (idx >= 4 && idx < 10 && xc * 6 + idx - 4 < 43) ? xc * 6 + idx - 4 : -1;
    const int dc = idx < 4 ? xc * 4 + idx : -1;
    constexpr unsigned HL = 147456, RD = HL + 8192;
    if (fc >= 0) {
      bar();
      const char* src = ((lane & 4) ? P.wu : P.wg) + fc * 64 + (lane & 3) * 16 + (size_t)(w * 64 + (lane >> 3)) * 2752;
#pragma unroll
      for (int i = 0; i < 8; ++i) glds(src + i * 8 * 2752, (unsigned)((w * 64 + i * 8) * 128));
      VMWAIT(0);
      bar();
      bf8 ff[16];
      const unsigned a0 = (16 * g + tq) * 128 + (tp >> 1) * 64 + wl * 16 + (tp & 1) * 8;
#pragma unroll
      for (int k = 0; k < 16; ++k) ff[k] = trfrag(a0 + (128 * (k >> 2) + 4 * (k & 3)) * 128, a0 + (128 * (k >> 2) + 4 * (k & 3) + 64) * 128);
      STAMP(10);
      SLEEP(H_DELAY);
      {
        u4 t;
        for (int it = 0; it < SPIN_CAP; ++it) {
          FENCE();
          t = ld16(ws, OFF_H + (xc % RH) * 8192 + tid * 16);
          if (__all(umax4(0, t) != SENT)) break;
          SLEEP(1);
        }
        lwr16(HL + tid * 16, t);
      }
      bar();
      STAMP(11);
      if (w < 4) {
        f4 hv[8];
#pragma unroll
        for (int i = 0; i < 8; ++i) hv[i] = __builtin_bit_cast(f4, lrd16(HL + i * 1024 + lane * 16));
        SB();
        const f4 s4v = ((hv[0] + hv[1]) + (hv[2] + hv[3])) + ((hv[4] + hv[5]) + (hv[6] + hv[7]));
        const float mean = sum_vg((s4v[0] + s4v[1]) + (s4v[2] + s4v[3])) * (1.f / 512.f);
        f4 q4 = {0, 0, 0, 0};
        f4 a[4] = {{0, 0, 0, 0}, {0, 0, 0, 0}, {0, 0, 0, 0}, {0, 0, 0, 0}};
#pragma unroll
        for (int u = 0; u < 4; ++u) {
          const f4 y0 = hv[2 * u] - mean, y1 = hv[2 * u + 1] - mean;
          const bf8 hb = asb(u4{pk(y0[0], y0[1]), pk(y0[2], y0[3]), pk(y1[0], y1[1]), pk(y1[2], y1[3])});
#pragma unroll
          for (int vv = 0; vv < 4; ++vv) a[vv] = mfma16(ff[u * 4 + vv], hb, a[vv]);
          q4 += y0 * y0; q4 += y1 * y1;
        }
        const float rs = __builtin_amdgcn_rsqf(sum_vg((q4[0] + q4[1]) + (q4[2] + q4[3])) * (1.f / 512.f) + 1e-5f);
        const f4 r = sel4(v, a[0], a[1], a[2], a[3]);
        float hh[4];
#pragma unroll
        for (int i = 0; i < 4; ++i) {
          const float z = sum_v(r[i]) * rs;
          auto sw = __builtin_amdgcn_permlane32_swap(asu(z), asu(z), false, false);
          const float gt = asf(sw[0]), up = asf(sw[1]);
          const float inner = 0.7978845608f * (gt + 0.044715f * gt * gt * gt);
          hh[i] = gt * __builtin_amdgcn_rcpf(1.f + __builtin_amdgcn_exp2f(-2.885390082f * inner)) * up;
        }
        const unsigned p0 = nosent(pk(hh[0], hh[1])), p1 = nosent(pk(hh[2], hh[3]));
        auto s0 = __builtin_amdgcn_permlane16_swap(p0, p0, false, false);
        auto s1 = __builtin_amdgcn_permlane16_swap(p1, p1, false, false);
        if ((lane >> 2) < RHH) st16(ws, OFF_HH + (lane >> 2) * 12288 + (fc * 4 + w) * 64 + bb * 16, u4{s0[0], s1[0], s0[1], s1[1]});
      }
      STAMP(12);
    } else if (dc >= 0) {
      bar();
      for (int i = w; i < 43; i += 8) glds(P.wd + (size_t)(i * 32 + (lane >> 1)) * 1024 + dc * 32 + (lane & 1) * 16, (unsigned)(i * 1024));
      VMWAIT(0);
      bar();
      if (w < 4) {
        bf8 ff[12];
#pragma unroll
        for (int k = 0; k < 12; ++k) {
          int row = 8 * (16 * (3 * w + (k >> 2)) + 4 * g + (k & 3)) + tq; row = row > 1371 ? 1368 : row;
          ff[k] = trfrag(row * 32 + tp * 8, row * 32 + tp * 8 + 128);
        }
        STAMP(10);
        SLEEP(HH_DELAY);
        u4 t[3], hr;
        for (int it = 0; it < SPIN_CAP; ++it) {
          FENCE();
          hr = ld16(ws, OFF_H + (xc % RH) * 8192 + (dc * 4 + g) * 64 + bb * 16);
#pragma unroll
          for (int i = 0; i < 3; ++i) t[i] = ld16(ws, OFF_HH + (xc % RHH) * 12288 + (3 * w + i) * 1024 + lane * 16);
          unsigned m = umax4(0, hr);
#pragma unroll
          for (int i = 0; i < 3; ++i) { const bool live = (3 * w + i) * 16 + (lane >> 2) < 172; if (!live) t[i] = u4{0, 0, 0, 0}; m = umax4(m, t[i]); }
          if (__all(m != SENT)) break;
          SLEEP(1);
        }
        STAMP(13);
        f4 a[4] = {{0, 0, 0, 0}, {0, 0, 0, 0}, {0, 0, 0, 0}, {0, 0, 0, 0}};
#pragma unroll
        for (int i = 0; i < 3; ++i)
#pragma unroll
          for (int vv = 0; vv < 4; ++vv) a[vv] = mfma16(ff[i * 4 + vv], asb(t[i]), a[vv]);
        f4 r = sel4(v, a[0], a[1], a[2], a[3]);
#pragma unroll
        for (int i = 0; i < 4; ++i) r[i] = sum_v(r[i]);
        if (w == 0) r += __builtin_bit_cast(f4, hr);
        if (v == 0) lwr16(RD + w * 256 + (g * 4 + bb) * 16, __builtin_bit_cast(u4, r));
      }
      bar();
      if (w == 0 && v == 0) {
        f4 o[4];
#pragma unroll
        for (int i = 0; i < 4; ++i) o[i] = __builtin_bit_cast(f4, lrd16(RD + i * 256 + (g * 4 + bb) * 16));
        SB();
        const f4 z = (o[0] + o[1]) + (o[2] + o[3]);
        *(u2*)(P.out + bb * 1024 + dc * 32 + g * 8) = u2{pk(z[0], z[1]), pk(z[2], z[3])};
      }
      STAMP(14);
    }
  }
}

extern "C" void kernel_launch(void* const* d_in, const int* in_sizes, int n_in, void* d_out, int out_size, void* d_ws, size_t ws_size,
                              hipStream_t stream) {
  Params p;
  p.x = (const char*)d_in[0]; p.ckv = (const char*)d_in[1]; p.wq = (const char*)d_in[2]; p.wuk = (const char*)d_in[3];
  p.wuv = (const char*)d_in[4]; p.wo = (const char*)d_in[5]; p.wg = (const char*)d_in[7]; p.wu = (const char*)d_in[8];
  p.wd = (const char*)d_in[9]; p.out = (char*)d_out; p.ws = (char*)d_ws;
  (void)hipFuncSetAttribute((const void*)fused, hipFuncAttributeMaxDynamicSharedMemorySize, 163840);
  fused<<<256, 512, 163840, stream>>>(p);
}
```

```cpp
#include <hip/hip_runtime.h>
#include <hip/hip_bf16.h>

typedef short s4 __attribute__((ext_vector_type(4)));
typedef __bf16 bf8 __attribute__((ext_vector_type(8)));
typedef __bf16 bf2 __attribute__((ext_vector_type(2)));
typedef float f2 __attribute__((ext_vector_type(2)));
typedef float f4 __attribute__((ext_vector_type(4)));
typedef float f16v __attribute__((ext_vector_type(16)));
typedef unsigned u2 __attribute__((ext_vector_type(2)));
typedef unsigned u4 __attribute__((ext_vector_type(4)));
#define LAS __attribute__((address_space(3)))
#define DI __device__ __forceinline__
typedef __amdgpu_buffer_rsrc_t rsrc_t;

constexpr unsigned OFF_E = 0, OFF_F2 = 0x4000, OFF_QP = 0x10000, OFF_Q = 0x700000, OFF_ML = 0xB0000, OFF_H = 0xC0000,
                   OFF_HH = 0xE0000, OFF_IN = 0x100000, IN_SZ = 0xA100, IN_AP = 0x8000, OFF_STAMP = 0x800000, WS_USED = 0x900000;
constexpr unsigned SENT = 0xFFFFFFFFu;
constexpr int SPIN_CAP = 200000;
#define AUX_ST 16
#define AUX_LD 16
constexpr int AUX_NT = 2;
#define RH 8
#define RHH 8
#define F1POS 6
#define Q_DELAY 0
#define QP_DELAY 8
#define ML_SLEEP 4
#define WO_ROT 0
#define WO_LD blds
#define F2LATE 1
#define PRE1 0
#define HOP1 0
#define O_DELAY 0
#define TPOS 10
#define PFR 1
#define QPOS 16
#define QT 360
#define PACE 8
#define TAIL_DELAY 90
#define H_DELAY 64
#define HH_DELAY 100
#define SB() __builtin_amdgcn_sched_barrier(0)
#define STR2(x) #x
#define STR(x) STR2(x)
#define FENCE() asm volatile("" ::: "memory")
#define SLEEP(n) __builtin_amdgcn_s_sleep(n)
#define ROLE_OF(b) (((b) >> 3) & 1)

#define STAMP(i)

DI void glds(const void* gsrc, unsigned lds_dst) {
  unsigned keep;
  asm volatile("s_mov_b32 %0, m0\n\ts_mov_b32 m0, %2\n\ts_nop 0\n\tglobal_load_lds_dwordx4 %1, off nt\n\ts_mov_b32 m0, %0"
               : "=&s"(keep) : "v"(gsrc), "s"(lds_dst) : "memory");
}
DI void blds(u4 rs, unsigned voff, unsigned soff, unsigned lds_dst) {
  unsigned keep;
  asm volatile("s_mov_b32 %0, m0\n\ts_mov_b32 m0, %4\n\ts_nop 0\n\tbuffer_load_dwordx4 %1, %2, %3 offen nt lds\n\ts_mov_b32 m0, %0"
               : "=&s"(keep) : "v"(voff), "s"(rs), "s"(soff), "s"(lds_dst) : "memory");
}
DI void blds0(u4 rs, unsigned voff, unsigned soff, unsigned lds_dst) {
  unsigned keep;
  asm volatile("s_mov_b32 %0, m0\n\ts_mov_b32 m0, %4\n\ts_nop 0\n\tbuffer_load_dwordx4 %1, %2, %3 offen lds\n\ts_mov_b32 m0, %0"
               : "=&s"(keep) : "v"(voff), "s"(rs), "s"(soff), "s"(lds_dst) : "memory");
}
DI u4 mkrs(const char* p, unsigned n) { const unsigned long long a = (unsigned long long)p; return u4{(unsigned)a, (unsigned)(a >> 32) & 0xFFFFu, n, 0x00020000u}; }
DI unsigned umax3(unsigned a, unsigned b, unsigned c) { unsigned m = a > b ? a : b; return m > c ? m : c; }
DI unsigned umax4(unsigned m, u4 v) { return umax3(umax3(m, v.x, v.y), v.z, v.w); }
#define VMWAIT(n) asm volatile("s_waitcnt vmcnt(" STR(n) ")" ::: "memory")
DI void bar() { asm volatile("s_waitcnt lgkmcnt(0)\n\ts_barrier" ::: "memory"); }
DI unsigned pk(float a, float b) { f2 x = {a, b}; bf2 y = __builtin_convertvector(x, bf2); return __builtin_bit_cast(unsigned, y); }
DI unsigned nosent(unsigned v) { return v < 0xFFFFFFFEu ? v : 0xFFFFFFFEu; }
DI float bflo(unsigned v) { return __builtin_bit_cast(float, v << 16); }
DI float bfhi(unsigned v) { return __builtin_bit_cast(float, v & 0xFFFF0000u); }
DI float asf(unsigned v) { return __builtin_bit_cast(float, v); }
DI unsigned asu(float v) { return __builtin_bit_cast(unsigned, v); }
template <int C> DI float dpp(float x) { return asf(__builtin_amdgcn_update_dpp(0u, asu(x), C, 0xF, 0xF, true)); }
DI float add16(float x) { auto s = __builtin_amdgcn_permlane16_swap(asu(x), asu(x), false, false); return asf(s[0]) + asf(s[1]); }
DI float add32(float x) { auto s = __builtin_amdgcn_permlane32_swap(asu(x), asu(x), false, false); return asf(s[0]) + asf(s[1]); }
DI float max16(float x) { auto s = __builtin_amdgcn_permlane16_swap(asu(x), asu(x), false, false); return fmaxf(asf(s[0]), asf(s[1])); }
DI float max32(float x) { auto s = __builtin_amdgcn_permlane32_swap(asu(x), asu(x), false, false); return fmaxf(asf(s[0]), asf(s[1])); }
DI float sum_v(float x) { x += dpp<0x128>(x); x += dpp<0x124>(x); return x; }
DI float sum_vg(float x) { return add32(add16(sum_v(x))); }
DI float sum_all(float x) { x += dpp<0xB1>(x); x += dpp<0x4E>(x); return sum_vg(x); }
DI u4 ld16(rsrc_t r, unsigned off) { return __builtin_amdgcn_raw_buffer_load_b128(r, off, 0, AUX_LD); }
DI u2 ld8(rsrc_t r, unsigned off) { return __builtin_amdgcn_raw_buffer_load_b64(r, off, 0, AUX_LD); }
DI void st16(rsrc_t r, unsigned off, u4 v) { __builtin_amdgcn_raw_buffer_store_b128(v, r, off, 0, AUX_ST); }
DI void st8(rsrc_t r, unsigned off, u2 v) { __builtin_amdgcn_raw_buffer_store_b64(v, r, off, 0, AUX_ST); }
DI bool ok4(u4 v) { return v.x != SENT && v.y != SENT && v.z != SENT && v.w != SENT; }
DI u4 lrd16(unsigned a) { return *(LAS u4*)a; }
DI void lwr16(unsigned a, u4 v) { *(LAS u4*)a = v; }
DI void lwr8(unsigned a, u2 v) { *(LAS u2*)a = v; }
DI void lwr4(unsigned a, unsigned v) { *(LAS unsigned*)a = v; }
DI float lrdf(unsigned a) { return *(LAS float*)a; }
DI bf8 trfrag(unsigned a0, unsigned a1) {
  s4 x = __builtin_amdgcn_ds_read_tr16_b64_v4i16((LAS s4*)a0), y = __builtin_amdgcn_ds_read_tr16_b64_v4i16((LAS s4*)a1);
  return __builtin_bit_cast(bf8, __builtin_shufflevector(x, y, 0, 1, 2, 3, 4, 5, 6, 7));
}
DI bf8 asb(u4 v) { return __builtin_bit_cast(bf8, v); }
DI f4 mfma16(bf8 a, bf8 b, f4 c) { return __builtin_amdgcn_mfma_f32_16x16x32_bf16(a, b, c, 0, 0, 0); }
DI f16v mfma32(bf8 a, bf8 b, f16v c) { return __builtin_amdgcn_mfma_f32_32x32x16_bf16(a, b, c, 0, 0, 0); }
DI f4 sel4(int v, f4 a, f4 b, f4 c, f4 d) { f4 x = (v & 1) ? b : a, y = (v & 1) ? d : c; return (v & 2) ? y : x; }
DI unsigned wsw(unsigned row) { return ((row & 3) | ((row >> 1) & 4)) << 5; }

struct Params {
  const char *x, *ckv, *wq, *wuk, *wuv, *wo, *wg, *wu, *wd;
  char *out, *ws;
};

#define EPOCH() const u2 e_ = *(LAS u2*)(131072 + (att ? w : 3) * 8192); const unsigned elo = __builtin_amdgcn_readfirstlane(e_.x) + 1, \
  ehi = __builtin_amdgcn_readfirstlane(e_.y) + (elo == 0)
DI bool flag_ok(u4 f, unsigned elo, unsigned ehi) { return __all(f.x == elo && f.y == ehi && f.z == elo && f.w == ehi); }

__global__ __launch_bounds__(512, 2) void fused(Params P) {
  const int tid = threadIdx.x, lane = tid & 63, w = __builtin_amdgcn_readfirstlane(tid >> 6);
  const int bid = blockIdx.x, xc = bid & 7, idx = bid >> 4;
  const int n = lane & 15, g = lane >> 4;
  const bool att = ROLE_OF(bid);
  STAMP(0);
  unsigned t00 = 0;
  const rsrc_t ws = __builtin_amdgcn_make_buffer_rsrc(P.ws, 0, WS_USED, 0x00020000);
  const int h = xc * 4 + (idx >> 2), j = idx & 3, cu = xc * 16 + idx;
  const char* cb = P.ckv + (size_t)cu * 262144;
  const u4 S4 = {SENT, SENT, SENT, SENT};
  if (att ? w < 4 : w == 3) blds0(mkrs(P.ws, 16), lane * 16, 0, 131072 + w * 8192);
  if (!att && w >= 4) blds0(mkrs(P.x, 4096), lane * 16, (w & 3) * 1024, 69632 + w * 1024);
  {
    const u4 rs = mkrs(att ? cb + w * 16384 : P.wq + (size_t)(j * 128 + w * 16) * 8192 + h * 256, 0x20000);
    const unsigned v0 = att ? lane * 16 : g * 8192 + ((n * 16) ^ (g << 5));
    const unsigned xm = att ? 15 : 2, xs = att ? 4 : 6, ss = att ? 1024 : 32768, cnt = att ? 16 : 4;
    unsigned ld = w * (att ? 16384 : 4096), so = 0;
#pragma nounroll
    for (unsigned i = 0; i < cnt; ++i) { blds(rs, v0 ^ ((i & xm) << xs), so, ld); so += ss; ld += 1024; if (i == TPOS) t00 = (unsigned)__builtin_amdgcn_s_memrealtime(); }
  }
  const int bb = lane & 3, v = (lane >> 2) & 3, tq = (lane & 15) >> 2, tp = lane & 3, wl = w & 3;
  if (!att) {
    constexpr unsigned XN = 65536, QB = XN + 2048, CT = 0, CX = 2048;
    if (w < 4) {
      const u4 rs = mkrs(P.wuk + (size_t)(j * 128 + w * 32) * 8192 + h * 256, 0x40000);
      const unsigned v0 = g * 8192 + ((n ^ g) * 16);
#pragma unroll
      for (int i = 0; i < 8; ++i) blds(rs, v0 ^ ((i & 6) << 5), i * 32768, 32768 + w * 8192 + i * 1024);
      VMWAIT(8);
    } else {
      VMWAIT(4);
      const u4 xr = lrd16(69632 + w * 1024 + lane * 16);
      STAMP(11);
      float xv[8] = {bflo(xr.x), bfhi(xr.x), bflo(xr.y), bfhi(xr.y), bflo(xr.z), bfhi(xr.z), bflo(xr.w), bfhi(xr.w)};
      float s = 0;
#pragma unroll
      for (int i = 0; i < 8; ++i) s += xv[i];
      const float mean = sum_all(s) * (1.f / 512.f);
      float q2 = 0;
#pragma unroll
      for (int i = 0; i < 8; ++i) { xv[i] -= mean; q2 += xv[i] * xv[i]; }
      const float rs = __builtin_amdgcn_rsqf(sum_all(q2) * (1.f / 512.f) + 1e-5f);
      if (g == j) lwr16(XN + wl * 320 + n * 16, u4{pk(xv[0] * rs, xv[1] * rs), pk(xv[2] * rs, xv[3] * rs), pk(xv[4] * rs, xv[5] * rs), pk(xv[6] * rs, xv[7] * rs)});
      STAMP(12);
      VMWAIT(0);
    }
    STAMP(1);
    bar();
    STAMP(2);
    EPOCH();
    {
      bf8 wa[4], xb[4];
#pragma unroll
      for (int s = 0; s < 4; ++s) {
        const int r0 = 32 * s + 8 * g + tq, r1 = r0 + 4;
        wa[s] = trfrag(r0 * 256 + ((w * 32 + tp * 8) ^ wsw(r0)), r1 * 256 + ((w * 32 + tp * 8) ^ wsw(r1)));
        xb[s] = asb(lrd16(XN + bb * 320 + s * 64 + g * 16));
      }
      SB();
      f4 a = {0, 0, 0, 0};
#pragma unroll
      for (int s = 0; s < 4; ++s) a = mfma16(wa[s], xb[s], a);
      if (v < 2) st16(ws, OFF_QP + cu * 4096 + (bb * 128 + w * 16 + 4 * g + 2 * v) * 8, v ? u4{asu(a[2]), elo, asu(a[3]), elo} : u4{asu(a[0]), elo, asu(a[1]), elo});
    }
    STAMP(3);
    bf8 uk[2][4];
    if (w >= 4) {
      u4 t[4];
      SLEEP(QP_DELAY);
#pragma nounroll
      for (int it = 0; it < SPIN_CAP; ++it) {
        FENCE();
        bool ok = true;
#pragma unroll
        for (int i = 0; i < 4; ++i) t[i] = ld16(ws, OFF_QP + (h * 4 + i) * 4096 + (tid - 256) * 16);
#pragma unroll
        for (int i = 0; i < 4; ++i) ok &= (t[i].y == elo && t[i].w == elo);
        if (__all(ok)) break;
        SLEEP(2);
      }
      lwr4(QB + wl * 320 + lane * 4, pk((asf(t[0].x) + asf(t[1].x)) + (asf(t[2].x) + asf(t[3].x)), (asf(t[0].z) + asf(t[1].z)) + (asf(t[2].z) + asf(t[3].z))));
    } else {
      VMWAIT(1);
#pragma unroll
      for (int t = 0; t < 2; ++t)
#pragma unroll
        for (int s = 0; s < 4; ++s) uk[t][s] = asb(lrd16(32768 + w * 8192 + (8 * (n >> 2) + 4 * t + (n & 3)) * 256 + (((4 * s + g) ^ n) * 16)));
    }
    bar();
    STAMP(4);
    if (w < 4) {
      bf8 qb[4];
#pragma unroll
      for (int s = 0; s < 4; ++s) qb[s] = asb(lrd16(QB + bb * 320 + s * 64 + g * 16));
      SB();
      f4 a0 = {0, 0, 0, 0}, a1 = {0, 0, 0, 0};
#pragma unroll
      for (int s = 0; s < 4; ++s) { a0 = mfma16(uk[0][s], qb[s], a0); a1 = mfma16(uk[1][s], qb[s], a1); }
      const float sc = 0.12751721f;
      const unsigned d3 = pk(a1[2] * sc, a1[3] * sc), ch = j * 16 + w * 4 + g, qo = OFF_Q + bb * 49152 + (h >> 3) * 12288;
      auto sw = __builtin_amdgcn_permlane16_swap(d3, d3, false, false);
      if (v == 0 || (v == 1 && !(g & 1)))
        st16(ws, v ? qo + 8192 + (h & 7) * 512 + ch * 8 : qo + (h & 7) * 1024 + ch * 16,
             v ? u4{sw[0], sw[1], ehi, elo} : u4{nosent(pk(a0[0] * sc, a0[1] * sc)), pk(a0[2] * sc, a0[3] * sc), pk(a1[0] * sc, a1[1] * sc), elo});
    }
    bar();
    STAMP(5);
    if (TAIL_DELAY) SLEEP(TAIL_DELAY);
    const unsigned inb = OFF_IN + cu * IN_SZ;
    const u2 xres = *(const u2*)(P.x + bb * 1024 + cu * 8);
#pragma unroll
    for (int i = 0; i < 5; ++i) st16(ws, inb + i * 8192 + tid * 16, S4);
    {
      unsigned off = WS_USED;
      if (w == 1 && lane < 4 * RH) off = OFF_H + (lane >> 2) * 8192 + cu * 64 + bb * 16;
      if (w == 2 && lane < 6 * RHH) off = OFF_HH + cu * 96 * RHH + lane * 16;
      st16(ws, off, S4);
    }
    FENCE();
    {
      const u4 ru = mkrs(P.wuv + (size_t)(j * 128 + w * 16) * 8192 + h * 256, 0x20000);
      const unsigned v0 = g * 8192 + ((n * 16) ^ (g << 5));
#pragma unroll
      for (int i = 0; i < 4; ++i) blds(ru, v0 ^ ((i & 2) << 6), i * 32768, 131072 + w * 4096 + i * 1024);
      const u4 ro = mkrs(P.wo + (size_t)h * 131072, 0x20000);
#pragma unroll
      for (int i = 0; i < 16; ++i) {
        const int row = ((i + WO_ROT * j) & 15) * 8 + w;
        WO_LD(ro, (lane * 16) ^ wsw(row), row * 1024, row * 1024);
        if (i == 3) {
          VMWAIT(8);
          bar();
          STAMP(13);
          if (tid < 8) st8(ws, OFF_F2 + tid * 1024 + cu * 8, u2{elo, ehi});
        }
      }
    }
    VMWAIT(0);
    bar();
    bf8 uv[4], wo[4][4];
#pragma unroll
    for (int s = 0; s < 4; ++s) {
      const int r0 = 32 * tq + 8 * s + 2 * g + (h & 1), r1 = r0 ^ 1;
      uv[s] = trfrag(131072 + r0 * 256 + ((w * 32 + tp * 8) ^ wsw(r0)), 131072 + r1 * 256 + ((w * 32 + tp * 8) ^ wsw(r1)));
    }
#pragma unroll
    for (int t = 0; t < 4; ++t)
#pragma unroll
      for (int s = 0; s < 4; ++s) {
        const int r0 = 32 * s + 8 * g + tq, r1 = r0 + 4;
        wo[t][s] = trfrag(r0 * 1024 + ((w * 128 + t * 32 + tp * 8) ^ wsw(r0)), r1 * 1024 + ((w * 128 + t * 32 + tp * 8) ^ wsw(r1)));
      }
    bar();
    STAMP(6);
    {
      u4 ml, o[4] = {S4, S4, S4, S4};
      const unsigned oa = inb + wl * 8192 + (lane >> 3) * 256 + (w >> 2) * 128 + (lane & 7) * 16;
      auto oload = [&]() {
#pragma unroll
        for (int i = 0; i < 4; ++i) if (umax4(0, o[i]) == SENT) o[i] = ld16(ws, oa + i * 2048);
      };
#pragma nounroll
      for (int it = 0; it < SPIN_CAP; ++it) {
        FENCE();
        ml = ld16(ws, OFF_ML + h * 2048 + (wl * 32 + (lane & 31)) * 16);
        if (HOP1) oload();
        if (__all(ml.y == elo && ml.w == elo)) break;
        SLEEP(ML_SLEEP);
      }
      FENCE();
      STAMP(7);
      if (O_DELAY) SLEEP(O_DELAY);
      oload();
      float mx = asf(ml.x);
      mx = fmaxf(mx, dpp<0xB1>(mx)); mx = fmaxf(mx, dpp<0x4E>(mx)); mx = fmaxf(mx, dpp<0x124>(mx)); mx = fmaxf(mx, dpp<0x128>(mx)); mx = max16(mx);
      const float ex = __builtin_amdgcn_exp2f(asf(ml.x) - mx);
      float L = ex * asf(ml.z);
      L += dpp<0xB1>(L); L += dpp<0x4E>(L); L = add16(sum_v(L));
      const float wt = ex * __builtin_amdgcn_rcpf(L);
      float wi[4];
#pragma unroll
      for (int i = 0; i < 4; ++i) wi[i] = asf(__builtin_amdgcn_ds_bpermute((8 * i + (lane >> 3)) * 4, asu(wt)));
      for (int it = 0; it < SPIN_CAP; ++it) {
        if (__all(umax4(umax4(umax4(umax4(0, o[0]), o[1]), o[2]), o[3]) != SENT)) break;
        FENCE();
        oload();
      }
      float acc[8] = {0, 0, 0, 0, 0, 0, 0, 0};
#pragma unroll
      for (int i = 0; i < 4; ++i) {
        acc[0] += wi[i] * bflo(o[i].x); acc[1] += wi[i] * bfhi(o[i].x); acc[2] += wi[i] * bflo(o[i].y); acc[3] += wi[i] * bfhi(o[i].y);
        acc[4] += wi[i] * bflo(o[i].z); acc[5] += wi[i] * bfhi(o[i].z); acc[6] += wi[i] * bflo(o[i].w); acc[7] += wi[i] * bfhi(o[i].w);
      }
      float t[4], u[2];
#pragma unroll
      for (int i = 0; i < 4; ++i) { auto s = __builtin_amdgcn_permlane32_swap(asu(acc[i]), asu(acc[i + 4]), false, false); t[i] = asf(s[0]) + asf(s[1]); }
#pragma unroll
      for (int i = 0; i < 2; ++i) { auto s = __builtin_amdgcn_permlane16_swap(asu(t[i]), asu(t[i + 2]), false, false); u[i] = asf(s[0]) + asf(s[1]); u[i] += dpp<0x128>(u[i]); }
      lwr4(CT + wl * 320 + (w >> 2) * 128 + (lane & 7) * 16 + g * 4, pk(u[0], u[1]));
    }
    bar();
    STAMP(8);
    {
      bf8 cb4[4];
#pragma unroll
      for (int s = 0; s < 4; ++s) cb4[s] = asb(lrd16(CT + bb * 320 + s * 64 + g * 16));
      SB();
      f4 a = {0, 0, 0, 0};
#pragma unroll
      for (int s = 0; s < 4; ++s) a = mfma16(uv[s], cb4[s], a);
      lwr8(CX + bb * 320 + (w * 16 + 4 * g) * 2, u2{pk(a[0], a[1]), pk(a[2], a[3])});
    }
    bar();
    {
      bf8 cbv[4];
#pragma unroll
      for (int s = 0; s < 4; ++s) cbv[s] = asb(lrd16(CX + bb * 320 + s * 64 + g * 16));
      SB();
      f4 a[4] = {{0, 0, 0, 0}, {0, 0, 0, 0}, {0, 0, 0, 0}, {0, 0, 0, 0}};
#pragma unroll
      for (int s = 0; s < 4; ++s)
#pragma unroll
        for (int t = 0; t < 4; ++t) a[t] = mfma16(wo[t][s], cbv[s], a[t]);
      const f4 r = sel4(v, a[0], a[1], a[2], a[3]);
      st16(ws, OFF_IN + IN_AP + (w * 16 + v * 4 + g) * IN_SZ + cu * 64 + bb * 16, u4{nosent(asu(r[0])), nosent(asu(r[1])), nosent(asu(r[2])), nosent(asu(r[3]))});
    }
    STAMP(9);
    if (cu == 0 && tid == 0) st8(ws, OFF_E, u2{elo, ehi});
    if (w == 7) {
      u4 t[8];
      for (int it = 0; it < SPIN_CAP; ++it) {
        FENCE();
        unsigned m = 0;
#pragma unroll
        for (int i = 0; i < 8; ++i) t[i] = ld16(ws, inb + IN_AP + i * 1024 + lane * 16);
#pragma unroll
        for (int i = 0; i < 8; ++i) m = umax4(m, t[i]);
        if (__all(m != SENT)) break;
      }
      f4 s = {0, 0, 0, 0};
#pragma unroll
      for (int i = 0; i < 8; ++i) s += __builtin_bit_cast(f4, t[i]);
      const float h0 = sum_vg(s[0]) + bflo(xres.x), h1 = sum_vg(s[1]) + bfhi(xres.x), h2 = sum_vg(s[2]) + bflo(xres.y), h3 = sum_vg(s[3]) + bfhi(xres.y);
      if (lane < 4 * RH) st16(ws, OFF_H + (lane >> 2) * 8192 + cu * 64 + bb * 16, u4{nosent(asu(h0)), nosent(asu(h1)), nosent(asu(h2)), nosent(asu(h3))});
    }
    STAMP(10);
  } else {
    const int b = xc >> 1;
    constexpr unsigned QL = 131072, PL = QL, MXL = QL + 8192, LSL = QL + 8192 + 1024;
    constexpr float TH = 40.f;
    if (w < 4) {
      lwr4(QL + (w * 8 + 7) * 1024 + lane * 16, SENT);
      bar();
      STAMP(1);
      __builtin_amdgcn_s_setprio(3);
      VMWAIT(QPOS);
      EPOCH();
      const int krow = 4 * (n & 3) + (n >> 2);
      unsigned qkb = (w * 16 + krow) * 1024 + g * 256 + krow * 16;
      bf8 a[16];
      {
#pragma nounroll
        for (int it = 0; it < 64 && (unsigned)__builtin_amdgcn_s_memrealtime() - t00 < QT; ++it) SLEEP(4);
        STAMP(2);
        u4 q[12];
        const unsigned qa = OFF_Q + b * 49152 + w * 12288 + lane * 16;
#pragma unroll
        for (int i = 0; i < 12; ++i) q[i] = ld16(ws, qa + i * 1024);
        VMWAIT(12);
#pragma unroll
        for (int s = 0; s < 16; ++s) a[s] = asb(lrd16(qkb ^ (s * 16)));
#pragma nounroll
        for (int it = 0; it < SPIN_CAP; ++it) {
          unsigned bad = 0;
#pragma unroll
          for (int i = 0; i < 12; ++i) bad |= __any(q[i].w != elo || (i >= 8 && q[i].z != ehi)) << i;
          if (!bad) break;
          FENCE();
#pragma unroll
          for (int i = 0; i < 12; ++i) if (bad >> i & 1) q[i] = ld16(ws, qa + i * 1024);
        }
#pragma unroll
        for (int i = 0; i < 4; ++i) {
          const unsigned row = w * 8 + i * 2 + (lane >> 5), xa = QL + row * 1024 + ((((lane & 31) * 2) ^ (row & 15)) * 16) + 12;
          lwr4(xa, q[8 + i].x); lwr4(xa ^ 16, q[8 + i].y);
        }
#pragma unroll
        for (int i = 0; i < 8; ++i) {
          const unsigned ma = QL + (w * 8 + i) * 1024 + ((lane ^ ((w * 8 + i) & 15)) * 16);
          lwr4(ma + 8, q[i].z); FENCE(); lwr8(ma, u2{q[i].x, q[i].y}); FENCE();
        }
      }
      STAMP(3);
      __builtin_amdgcn_s_setprio(0);
#pragma nounroll
      for (int it = 0; it < SPIN_CAP; ++it) {
        FENCE();
        unsigned m = 0;
#pragma unroll
        for (int i = 0; i < 4; ++i) { const unsigned x = *(LAS unsigned*)(QL + (i * 8 + 7) * 1024 + lane * 16); m = x > m ? x : m; }
        if (__all(m != SENT)) break;
      }
      FENCE();
      u4 f2;
      unsigned pf0, pf1;
      bf8 qf[2][16];
#pragma unroll
      for (int s = 0; s < 16; ++s)
#pragma unroll
        for (int ht = 0; ht < 2; ++ht) qf[ht][s] = asb(lrd16(QL + (ht * 16 + n) * 1024 + g * 256 + ((s ^ n) * 16)));
      SB();
      float m0 = 0.f, m1 = 0.f, l0 = 0.f, l1 = 0.f;
      unsigned pa = PL + ((w * 2 + (g >> 1)) * 32 + n) * 16 + 8 * (g & 1), mxa = MXL;
      const unsigned lsa = LSL + n * 64 + (w * 4 + g) * 4;
#pragma unroll
      for (int r = 0; r < 4; ++r) {
        f4 s0 = {0, 0, 0, 0}, s1 = {0, 0, 0, 0};
#pragma unroll
        for (int s = 0; s < 16; ++s) { s0 = mfma16(a[s], qf[0][s], s0); s1 = mfma16(a[s], qf[1][s], s1); }
        SB();
        if (r == 0) {
          bar();
        }
        float t0, t1;
        auto emit = [&]() {
          f4 p0, p1;
#pragma unroll
          for (int i = 0; i < 4; ++i) { p0[i] = __builtin_amdgcn_exp2f(s0[i] - m0); p1[i] = __builtin_amdgcn_exp2f(s1[i] - m1); }
          lwr8(pa, u2{pk(p0[0], p0[1]), pk(p0[2], p0[3])});
          lwr8(pa + 256, u2{pk(p1[0], p1[1]), pk(p1[2], p1[3])});
          t0 = l0 + ((p0[0] + p0[1]) + (p0[2] + p0[3])); t1 = l1 + ((p1[0] + p1[1]) + (p1[2] + p1[3]));
          if (r == 3) { lwr4(lsa, asu(t0)); lwr4(lsa + 1024, asu(t1)); }
        };
        emit();
        {
          const float x0 = fmaxf(fmaxf(s0[0], s0[1]), fmaxf(s0[2], s0[3])), x1 = fmaxf(fmaxf(s1[0], s1[1]), fmaxf(s1[2], s1[3]));
          auto sw = __builtin_amdgcn_permlane16_swap(asu(x0), asu(x1), false, false);
          const float x = max32(fmaxf(asf(sw[0]), asf(sw[1])));
          lwr4(mxa + ((lane & 31) * 4 + w) * 4, asu(x));
        }
        if (r == 3 && w == 0) {
#pragma nounroll
          for (int it = 0; it < SPIN_CAP && !flag_ok(f2, elo, ehi); ++it) { FENCE(); f2 = ld16(ws, OFF_F2 + xc * 1024 + lane * 16); }
        }
        bar();
        if (r == PFR) {
          const rsrc_t rf = __builtin_amdgcn_make_buffer_rsrc((void*)(w == 1 ? P.wg : w == 2 ? P.wu : P.wd), 0, w ? 1409024 : 0, 0x00020000);
          const unsigned po = w < 3 ? (idx * 32 + (lane >> 2)) * 2752 + xc * 384 + (bb < 3 ? bb * 128 : 380) : (idx * 86 + lane) * 1024 + xc * 128;
          pf0 = __builtin_amdgcn_raw_buffer_load_b32(rf, po, 0, 0);
          pf1 = __builtin_amdgcn_raw_buffer_load_b32(rf, w < 3 ? po + 16 * 2752 : lane < 22 ? po + 65536 : 1409024, 0, 0);
        }
        if (r == 1) f2 = ld16(ws, w == 0 ? OFF_F2 + xc * 1024 + lane * 16 : WS_USED);
        if (r == 2) __builtin_amdgcn_s_setprio(3);
        const u4 ma = lrd16(mxa + n * 16), mb = lrd16(mxa + (16 + n) * 16);
        if (PRE1 ? (r == 1 || r == 2) : r < 3) {
          qkb ^= 65536;
          asm volatile("" : "+v"(qkb));
#pragma unroll
          for (int s = 0; s < 16; ++s) a[s] = asb(lrd16(qkb ^ (s * 16)));
        }
        SB();
        const float n0 = fmaxf(fmaxf(asf(ma.x), asf(ma.y)), fmaxf(asf(ma.z), asf(ma.w)));
        const float n1 = fmaxf(fmaxf(asf(mb.x), asf(mb.y)), fmaxf(asf(mb.z), asf(mb.w)));
        if (__builtin_expect(__any(r == 0 ? !(fabsf(n0) <= TH && fabsf(n1) <= TH) : (n0 > m0 + TH || n1 > m1 + TH)), 0)) {
          const float k0 = r == 0 ? n0 : fmaxf(m0, n0), k1 = r == 0 ? n1 : fmaxf(m1, n1);
          if (r > 0) { l0 *= __builtin_amdgcn_exp2f(m0 - k0); l1 *= __builtin_amdgcn_exp2f(m1 - k1); }
          m0 = k0; m1 = k1;
          bar();
          emit();
          bar();
        }
        l0 = t0; l1 = t1;
        pa ^= 4096; mxa ^= 512;
        if (r == 0 || r == 3) STAMP(5 + r);
      }
      if (w == 0) {
        f4 s = {0, 0, 0, 0};
#pragma unroll
        for (int i = 0; i < 4; ++i) s += __builtin_bit_cast(f4, lrd16(LSL + (lane & 31) * 64 + i * 16));
        if (lane < 32) st16(ws, OFF_ML + lane * 2048 + cu * 16, u4{asu(g ? m1 : m0), elo, asu((s[0] + s[1]) + (s[2] + s[3])), elo});
      } else asm volatile("" :: "v"(pf0), "v"(pf1));
      __builtin_amdgcn_s_setprio(0);
    } else {
      const int hf = lane >> 5, blk = g & 1;
      u4 Ld[32];
      const rsrc_t cr = __builtin_amdgcn_make_buffer_rsrc((void*)cb, 0, 262144, 0x00020000);
      const unsigned lp = (128 + g) * 1024 + wl * 256 + n * 16;
      bar();
#pragma unroll
      for (int i = 0; i < 32; ++i) { VMWAIT(PACE); Ld[i] = __builtin_amdgcn_raw_buffer_load_b128(cr, lp, i * 4096, AUX_NT); }
      VMWAIT(32);
      STAMP(1);
      f16v O[4];
#pragma unroll
      for (int c = 0; c < 4; ++c)
#pragma unroll
        for (int i = 0; i < 16; ++i) O[c][i] = 0.f;
      float mr = 0.f;
      unsigned trb[2];
#pragma unroll
      for (int t = 0; t < 2; ++t) { const int row = 4 * tq + 2 * hf + t; trb[t] = row * 1024 + wl * 256 + (((2 * blk + (tp >> 1)) ^ row) * 16) + 8 * (tp & 1); }
      const unsigned wb = g * 1024 + wl * 256 + ((n ^ g) * 16);
      bf8 a[4][4];
      auto rd = [&](int r, int ks) {
#pragma unroll
        for (int c = 0; c < 4; ++c) a[ks][c] = trfrag((r & 1) * 65536 + ks * 16384 + (trb[0] ^ (c * 64)), (r & 1) * 65536 + ks * 16384 + (trb[1] ^ (c * 64)));
      };
      bar();
      rd(0, 0);
#pragma unroll
      for (int r = 0; r < 4; ++r) {
        asm volatile("s_waitcnt lgkmcnt(8)\n\ts_barrier" ::: "memory");
        const u4 ma = lrd16(MXL + (r & 1) * 512 + (lane & 31) * 16);
        bf8 pb[4];
#pragma unroll
        for (int ks = 0; ks < (r ? 4 : 1); ++ks) pb[ks] = asb(lrd16(PL + (r & 1) * 4096 + (ks * 64 + lane) * 16));
        SB();
        const float n0 = fmaxf(fmaxf(asf(ma.x), asf(ma.y)), fmaxf(asf(ma.z), asf(ma.w)));
        if (__builtin_expect(__any(r == 0 ? !(fabsf(n0) <= TH) : n0 > mr + TH), 0)) {
          const float k0 = r == 0 ? n0 : fmaxf(mr, n0), al = __builtin_amdgcn_exp2f(mr - k0);
          mr = k0;
          bar();
          if (r > 0) {
#pragma unroll
            for (int i = 0; i < 16; ++i) {
              const float z = asf(__builtin_amdgcn_ds_bpermute((8 * (i >> 2) + 4 * hf + (i & 3)) * 4, asu(al)));
#pragma unroll
              for (int c = 0; c < 4; ++c) O[c][i] *= z;
            }
          }
          bar();
#pragma unroll
          for (int ks = 0; ks < (r ? 4 : 1); ++ks) pb[ks] = asb(lrd16(PL + (r & 1) * 4096 + (ks * 64 + lane) * 16));
        }
#pragma unroll
        for (int ks = 0; ks < 4; ++ks) {
          if (r < 2) {
            if (ks < 3) { rd(r, ks + 1); if (r == 0) pb[ks + 1] = asb(lrd16(PL + (ks * 64 + 64 + lane) * 16)); }
          }
          SB();
#pragma unroll
          for (int c = 0; c < 4; ++c) {
            O[c] = mfma32(pb[ks], a[ks][c], O[c]);
            if (r < 2) { SB(); lwr16((r & 1) * 65536 + (ks * 16 + 4 * c) * 1024 + (wb ^ (c * 64)), Ld[r * 16 + ks * 4 + c]); SB(); }
          }
          SB();
          if (r == 0 && ks == 3) rd(1, 0);
          if (r == 1 || r == 2) rd(r + 1, ks);
          SB();
        }
        if (r == 0 || r == 3) STAMP(5 + r);
      }
      const bool odd = lane & 1;
      const unsigned ob = OFF_IN + (hf * 16 + (lane & 1) * 4 + wl) * IN_SZ + cu * 256 + (lane & 30) * 8;
#pragma unroll
      for (int i = 0; i < 16; i += 2) {
        const unsigned a0 = nosent(pk(O[0][i], O[1][i])), a1 = nosent(pk(O[2][i], O[3][i]));
        const unsigned b0 = nosent(pk(O[0][i + 1], O[1][i + 1])), b1 = nosent(pk(O[2][i + 1], O[3][i + 1]));
        const unsigned y0 = __builtin_amdgcn_update_dpp(0u, odd ? a0 : b0, 0xB1, 0xF, 0xF, true), y1 = __builtin_amdgcn_update_dpp(0u, odd ? a1 : b1, 0xB1, 0xF, 0xF, true);
        __builtin_amdgcn_raw_buffer_store_b128(u4{odd ? b0 : a0, odd ? b1 : a1, y0, y1}, ws, ob, (8 * (i >> 2) + (i & 3)) * 4 * IN_SZ, AUX_ST);
      }
    }
    STAMP(9);
    const int fc = (idx >= 4 && idx < 10 && xc * 6 + idx - 4 < 43) ? xc * 6 + idx - 4 : -1;
    const int dc = idx < 4 ? xc * 4 + idx : -1;
    constexpr unsigned HL = 147456, RD = HL + 8192;
    if (fc >= 0) {
      bar();
      const char* src = ((lane & 4) ? P.wu : P.wg) + fc * 64 + (lane & 3) * 16 + (size_t)(w * 64 + (lane >> 3)) * 2752;
#pragma unroll
      for (int i = 0; i < 8; ++i) glds(src + i * 8 * 2752, (unsigned)((w * 64 + i * 8) * 128));
      VMWAIT(0);
      bar();
      bf8 ff[16];
      const unsigned a0 = (16 * g + tq) * 128 + (tp >> 1) * 64 + wl * 16 + (tp & 1) * 8;
#pragma unroll
      for (int k = 0; k < 16; ++k) ff[k] = trfrag(a0 + (128 * (k >> 2) + 4 * (k & 3)) * 128, a0 + (128 * (k >> 2) + 4 * (k & 3) + 64) * 128);
      STAMP(10);
      SLEEP(H_DELAY);
      {
        u4 t;
        for (int it = 0; it < SPIN_CAP; ++it) {
          FENCE();
          t = ld16(ws, OFF_H + (xc % RH) * 8192 + tid * 16);
          if (__all(umax4(0, t) != SENT)) break;
          SLEEP(1);
        }
        lwr16(HL + tid * 16, t);
      }
      bar();
      STAMP(11);
      if (w < 4) {
        f4 hv[8];
#pragma unroll
        for (int i = 0; i < 8; ++i) hv[i] = __builtin_bit_cast(f4, lrd16(HL + i * 1024 + lane * 16));
        SB();
        const f4 s4v = ((hv[0] + hv[1]) + (hv[2] + hv[3])) + ((hv[4] + hv[5]) + (hv[6] + hv[7]));
        const float mean = sum_vg((s4v[0] + s4v[1]) + (s4v[2] + s4v[3])) * (1.f / 512.f);
        f4 q4 = {0, 0, 0, 0};
        f4 a[4] = {{0, 0, 0, 0}, {0, 0, 0, 0}, {0, 0, 0, 0}, {0, 0, 0, 0}};
#pragma unroll
        for (int u = 0; u < 4; ++u) {
          const f4 y0 = hv[2 * u] - mean, y1 = hv[2 * u + 1] - mean;
          const bf8 hb = asb(u4{pk(y0[0], y0[1]), pk(y0[2], y0[3]), pk(y1[0], y1[1]), pk(y1[2], y1[3])});
#pragma unroll
          for (int vv = 0; vv < 4; ++vv) a[vv] = mfma16(ff[u * 4 + vv], hb, a[vv]);
          q4 += y0 * y0; q4 += y1 * y1;
        }
        const float rs = __builtin_amdgcn_rsqf(sum_vg((q4[0] + q4[1]) + (q4[2] + q4[3])) * (1.f / 512.f) + 1e-5f);
        const f4 r = sel4(v, a[0], a[1], a[2], a[3]);
        float hh[4];
#pragma unroll
        for (int i = 0; i < 4; ++i) {
          const float z = sum_v(r[i]) * rs;
          auto sw = __builtin_amdgcn_permlane32_swap(asu(z), asu(z), false, false);
          const float gt = asf(sw[0]), up = asf(sw[1]);
          const float inner = 0.7978845608f * (gt + 0.044715f * gt * gt * gt);
          hh[i] = gt * __builtin_amdgcn_rcpf(1.f + __builtin_amdgcn_exp2f(-2.885390082f * inner)) * up;
        }
        const unsigned p0 = nosent(pk(hh[0], hh[1])), p1 = nosent(pk(hh[2], hh[3]));
        auto s0 = __builtin_amdgcn_permlane16_swap(p0, p0, false, false);
        auto s1 = __builtin_amdgcn_permlane16_swap(p1, p1, false, false);
        if ((lane >> 2) < RHH) st16(ws, OFF_HH + (lane >> 2) * 12288 + (fc * 4 + w) * 64 + bb * 16, u4{s0[0], s1[0], s0[1], s1[1]});
      }
      STAMP(12);
    } else if (dc >= 0) {
      bar();
      for (int i = w; i < 43; i += 8) glds(P.wd + (size_t)(i * 32 + (lane >> 1)) * 1024 + dc * 32 + (lane & 1) * 16, (unsigned)(i * 1024));
      VMWAIT(0);
      bar();
      if (w < 4) {
        bf8 ff[12];
#pragma unroll
        for (int k = 0; k < 12; ++k) {
          int row = 8 * (16 * (3 * w + (k >> 2)) + 4 * g + (k & 3)) + tq; row = row > 1371 ? 1368 : row;
          ff[k] = trfrag(row * 32 + tp * 8, row * 32 + tp * 8 + 128);
        }
        STAMP(10);
        SLEEP(HH_DELAY);
        u4 t[3], hr;
        for (int it = 0; it < SPIN_CAP; ++it) {
          FENCE();
          hr = ld16(ws, OFF_H + (xc % RH) * 8192 + (dc * 4 + g) * 64 + bb * 16);
#pragma unroll
          for (int i = 0; i < 3; ++i) t[i] = ld16(ws, OFF_HH + (xc % RHH) * 12288 + (3 * w + i) * 1024 + lane * 16);
          unsigned m = umax4(0, hr);
#pragma unroll
          for (int i = 0; i < 3; ++i) { const bool live = (3 * w + i) * 16 + (lane >> 2) < 172; if (!live) t[i] = u4{0, 0, 0, 0}; m = umax4(m, t[i]); }
          if (__all(m != SENT)) break;
          SLEEP(1);
        }
        STAMP(13);
        f4 a[4] = {{0, 0, 0, 0}, {0, 0, 0, 0}, {0, 0, 0, 0}, {0, 0, 0, 0}};
#pragma unroll
        for (int i = 0; i < 3; ++i)
#pragma unroll
          for (int vv = 0; vv < 4; ++vv) a[vv] = mfma16(ff[i * 4 + vv], asb(t[i]), a[vv]);
        f4 r = sel4(v, a[0], a[1], a[2], a[3]);
#pragma unroll
        for (int i = 0; i < 4; ++i) r[i] = sum_v(r[i]);
        if (w == 0) r += __builtin_bit_cast(f4, hr);
        if (v == 0) lwr16(RD + w * 256 + (g * 4 + bb) * 16, __builtin_bit_cast(u4, r));
      }
      bar();
      if (w == 0 && v == 0) {
        f4 o[4];
#pragma unroll
        for (int i = 0; i < 4; ++i) o[i] = __builtin_bit_cast(f4, lrd16(RD + i * 256 + (g * 4 + bb) * 16));
        SB();
        const f4 z = (o[0] + o[1]) + (o[2] + o[3]);
        *(u2*)(P.out + bb * 1024 + dc * 32 + g * 8) = u2{pk(z[0], z[1]), pk(z[2], z[3])};
      }
      STAMP(14);
    }
  }
}

extern "C" void kernel_launch(void* const* d_in, const int* in_sizes, int n_in, void* d_out, int out_size, void* d_ws, size_t ws_size,
                              hipStream_t stream) {
  Params p;
  p.x = (const char*)d_in[0]; p.ckv = (const char*)d_in[1]; p.wq = (const char*)d_in[2]; p.wuk = (const char*)d_in[3];
  p.wuv = (const char*)d_in[4]; p.wo = (const char*)d_in[5]; p.wg = (const char*)d_in[7]; p.wu = (const char*)d_in[8];
  p.wd = (const char*)d_in[9]; p.out = (char*)d_out; p.ws = (char*)d_ws;
  (void)hipFuncSetAttribute((const void*)fused, hipFuncAttributeMaxDynamicSharedMemorySize, 163840);
  fused<<<256, 512, 163840, stream>>>(p);
}
```

```cpp
#include <hip/hip_runtime.h>
#include <hip/hip_bf16.h>

typedef short s4 __attribute__((ext_vector_type(4)));
typedef __bf16 bf8 __attribute__((ext_vector_type(8)));
typedef __bf16 bf2 __attribute__((ext_vector_type(2)));
typedef float f2 __attribute__((ext_vector_type(2)));
typedef float f4 __attribute__((ext_vector_type(4)));
typedef float f16v __attribute__((ext_vector_type(16)));
typedef unsigned u2 __attribute__((ext_vector_type(2)));
typedef unsigned u4 __attribute__((ext_vector_type(4)));
#define LAS __attribute__((address_space(3)))
#define DI __device__ __forceinline__
typedef __amdgpu_buffer_rsrc_t rsrc_t;

constexpr unsigned OFF_E = 0, OFF_F1 = 0x1000, OFF_F2 = 0x4000, OFF_QP = 0x10000, OFF_Q = 0x90000, OFF_ML = 0xB0000, OFF_H = 0xC0000,
                   OFF_HH = 0xE0000, OFF_IN = 0x100000, IN_SZ = 0xA100, IN_AP = 0x8000, OFF_STAMP = 0x800000, WS_USED = 0x900000;
constexpr unsigned SENT = 0xFFFFFFFFu;
constexpr int SPIN_CAP = 200000;
#define AUX_ST 16
#define AUX_LD 16
constexpr int AUX_NT = 2;
#define RH 8
#define RHH 8
#define F1POS 6
#define Q_DELAY 0
#define QP_DELAY 8
#define ML_SLEEP 4
#define WO_ROT 0
#define WO_LD blds
#define PACE 14
#define TAIL_DELAY 70
#define H_DELAY 64
#define HH_DELAY 100
#define SB() __builtin_amdgcn_sched_barrier(0)
#define STR2(x) #x
#define STR(x) STR2(x)
#define FENCE() asm volatile("" ::: "memory")
#define SLEEP(n) __builtin_amdgcn_s_sleep(n)
#define ROLE_OF(b) (((b) >> 3) & 1)

#define STAMP(i)

DI void glds(const void* gsrc, unsigned lds_dst) {
  unsigned keep;
  asm volatile("s_mov_b32 %0, m0\n\ts_mov_b32 m0, %2\n\ts_nop 0\n\tglobal_load_lds_dwordx4 %1, off nt\n\ts_mov_b32 m0, %0"
               : "=&s"(keep) : "v"(gsrc), "s"(lds_dst) : "memory");
}
DI void blds(u4 rs, unsigned voff, unsigned soff, unsigned lds_dst) {
  unsigned keep;
  asm volatile("s_mov_b32 %0, m0\n\ts_mov_b32 m0, %4\n\ts_nop 0\n\tbuffer_load_dwordx4 %1, %2, %3 offen nt lds\n\ts_mov_b32 m0, %0"
               : "=&s"(keep) : "v"(voff), "s"(rs), "s"(soff), "s"(lds_dst) : "memory");
}
DI void blds0(u4 rs, unsigned voff, unsigned soff, unsigned lds_dst) {
  unsigned keep;
  asm volatile("s_mov_b32 %0, m0\n\ts_mov_b32 m0, %4\n\ts_nop 0\n\tbuffer_load_dwordx4 %1, %2, %3 offen lds\n\ts_mov_b32 m0, %0"
               : "=&s"(keep) : "v"(voff), "s"(rs), "s"(soff), "s"(lds_dst) : "memory");
}
DI u4 mkrs(const char* p, unsigned n) { const unsigned long long a = (unsigned long long)p; return u4{(unsigned)a, (unsigned)(a >> 32) & 0xFFFFu, n, 0x00020000u}; }
DI unsigned umax3(unsigned a, unsigned b, unsigned c) { unsigned m = a > b ? a : b; return m > c ? m : c; }
DI unsigned umax4(unsigned m, u4 v) { return umax3(umax3(m, v.x, v.y), v.z, v.w); }
#define VMWAIT(n) asm volatile("s_waitcnt vmcnt(" STR(n) ")" ::: "memory")
DI void bar() { asm volatile("s_waitcnt lgkmcnt(0)\n\ts_barrier" ::: "memory"); }
DI unsigned pk(float a, float b) { f2 x = {a, b}; bf2 y = __builtin_convertvector(x, bf2); return __builtin_bit_cast(unsigned, y); }
DI unsigned nosent(unsigned v) { return v < 0xFFFFFFFEu ? v : 0xFFFFFFFEu; }
DI float bflo(unsigned v) { return __builtin_bit_cast(float, v << 16); }
DI float bfhi(unsigned v) { return __builtin_bit_cast(float, v & 0xFFFF0000u); }
DI float asf(unsigned v) { return __builtin_bit_cast(float, v); }
DI unsigned asu(float v) { return __builtin_bit_cast(unsigned, v); }
template <int C> DI float dpp(float x) { return asf(__builtin_amdgcn_update_dpp(0u, asu(x), C, 0xF, 0xF, true)); }
DI float add16(float x) { auto s = __builtin_amdgcn_permlane16_swap(asu(x), asu(x), false, false); return asf(s[0]) + asf(s[1]); }
DI float add32(float x) { auto s = __builtin_amdgcn_permlane32_swap(asu(x), asu(x), false, false); return asf(s[0]) + asf(s[1]); }
DI float max16(float x) { auto s = __builtin_amdgcn_permlane16_swap(asu(x), asu(x), false, false); return fmaxf(asf(s[0]), asf(s[1])); }
DI float max32(float x) { auto s = __builtin_amdgcn_permlane32_swap(asu(x), asu(x), false, false); return fmaxf(asf(s[0]), asf(s[1])); }
DI float sum_v(float x) { x += dpp<0x128>(x); x += dpp<0x124>(x); return x; }
DI float sum_vg(float x) { return add32(add16(sum_v(x))); }
DI float sum_all(float x) { x += dpp<0xB1>(x); x += dpp<0x4E>(x); return sum_vg(x); }
DI u4 ld16(rsrc_t r, unsigned off) { return __builtin_amdgcn_raw_buffer_load_b128(r, off, 0, AUX_LD); }
DI u2 ld8(rsrc_t r, unsigned off) { return __builtin_amdgcn_raw_buffer_load_b64(r, off, 0, AUX_LD); }
DI void st16(rsrc_t r, unsigned off, u4 v) { __builtin_amdgcn_raw_buffer_store_b128(v, r, off, 0, AUX_ST); }
DI void st8(rsrc_t r, unsigned off, u2 v) { __builtin_amdgcn_raw_buffer_store_b64(v, r, off, 0, AUX_ST); }
DI bool ok4(u4 v) { return v.x != SENT && v.y != SENT && v.z != SENT && v.w != SENT; }
DI u4 lrd16(unsigned a) { return *(LAS u4*)a; }
DI void lwr16(unsigned a, u4 v) { *(LAS u4*)a = v; }
DI void lwr8(unsigned a, u2 v) { *(LAS u2*)a = v; }
DI void lwr4(unsigned a, unsigned v) { *(LAS unsigned*)a = v; }
DI float lrdf(unsigned a) { return *(LAS float*)a; }
DI bf8 trfrag(unsigned a0, unsigned a1) {
  s4 x = __builtin_amdgcn_ds_read_tr16_b64_v4i16((LAS s4*)a0), y = __builtin_amdgcn_ds_read_tr16_b64_v4i16((LAS s4*)a1);
  return __builtin_bit_cast(bf8, __builtin_shufflevector(x, y, 0, 1, 2, 3, 4, 5, 6, 7));
}
DI bf8 asb(u4 v) { return __builtin_bit_cast(bf8, v); }
DI f4 mfma16(bf8 a, bf8 b, f4 c) { return __builtin_amdgcn_mfma_f32_16x16x32_bf16(a, b, c, 0, 0, 0); }
DI f16v mfma32(bf8 a, bf8 b, f16v c) { return __builtin_amdgcn_mfma_f32_32x32x16_bf16(a, b, c, 0, 0, 0); }
DI f4 sel4(int v, f4 a, f4 b, f4 c, f4 d) { f4 x = (v & 1) ? b : a, y = (v & 1) ? d : c; return (v & 2) ? y : x; }
DI unsigned wsw(unsigned row) { return ((row & 3) | ((row >> 1) & 4)) << 5; }

struct Params {
  const char *x, *ckv, *wq, *wuk, *wuv, *wo, *wg, *wu, *wd;
  char *out, *ws;
};

#define EPOCH() const u2 e_ = *(LAS u2*)(131072 + (att ? w : 3) * 8192); const unsigned elo = __builtin_amdgcn_readfirstlane(e_.x) + 1, \
  ehi = __builtin_amdgcn_readfirstlane(e_.y) + (elo == 0)
DI bool flag_ok(u4 f, unsigned elo, unsigned ehi) { return __all(f.x == elo && f.y == ehi && f.z == elo && f.w == ehi); }

__global__ __launch_bounds__(512, 2) void fused(Params P) {
  const int tid = threadIdx.x, lane = tid & 63, w = __builtin_amdgcn_readfirstlane(tid >> 6);
  const int bid = blockIdx.x, xc = bid & 7, idx = bid >> 4;
  const int n = lane & 15, g = lane >> 4;
  const bool att = ROLE_OF(bid);
  STAMP(0);
  const rsrc_t ws = __builtin_amdgcn_make_buffer_rsrc(P.ws, 0, WS_USED, 0x00020000);
  const int h = xc * 4 + (idx >> 2), j = idx & 3, cu = xc * 16 + idx;
  const char* cb = P.ckv + (size_t)cu * 262144;
  const u4 S4 = {SENT, SENT, SENT, SENT};
  if (!att && w == 3) st16(ws, OFF_Q + g * 32768 + h * 1024 + j * 256 + n * 16, S4);
  FENCE();
  if (att ? w < 4 : w == 3) blds0(mkrs(P.ws, 16), lane * 16, 0, 131072 + w * 8192);
  if (!att && w >= 4) blds0(mkrs(P.x, 4096), lane * 16, (w & 3) * 1024, 69632 + w * 1024);
  {
    const u4 rs = mkrs(att ? cb + w * 16384 : P.wq + (size_t)(j * 128 + w * 16) * 8192 + h * 256, 0x20000);
    const unsigned v0 = att ? lane * 16 : g * 8192 + ((n * 16) ^ (g << 5));
    const unsigned xm = att ? 15 : 2, xs = att ? 4 : 6, ss = att ? 1024 : 32768, cnt = att ? 16 : 4;
    unsigned ld = w * (att ? 16384 : 4096), so = 0;
#pragma nounroll
    for (unsigned i = 0; i < cnt; ++i) { blds(rs, v0 ^ ((i & xm) << xs), so, ld); so += ss; ld += 1024; }
  }
  const int bb = lane & 3, v = (lane >> 2) & 3, tq = (lane & 15) >> 2, tp = lane & 3, wl = w & 3;
  if (!att) {
    constexpr unsigned XN = 65536, QB = XN + 2048, CT = 0, CX = 2048;
    if (w < 4) {
      const u4 rs = mkrs(P.wuk + (size_t)(j * 128 + w * 32) * 8192 + h * 256, 0x40000);
      const unsigned v0 = g * 8192 + ((n ^ g) * 16);
#pragma unroll
      for (int i = 0; i < 8; ++i) blds(rs, v0 ^ ((i & 6) << 5), i * 32768, 32768 + w * 8192 + i * 1024);
      if (w == 3) {
        VMWAIT(12);
        EPOCH();
        if (lane < 8) st8(ws, OFF_F1 + lane * 1024 + cu * 8, u2{elo, ehi});
        VMWAIT(9);
      } else VMWAIT(8);
    } else {
      VMWAIT(4);
      const u4 xr = lrd16(69632 + w * 1024 + lane * 16);
      STAMP(11);
      float xv[8] = {bflo(xr.x), bfhi(xr.x), bflo(xr.y), bfhi(xr.y), bflo(xr.z), bfhi(xr.z), bflo(xr.w), bfhi(xr.w)};
      float s = 0;
#pragma unroll
      for (int i = 0; i < 8; ++i) s += xv[i];
      const float mean = sum_all(s) * (1.f / 512.f);
      float q2 = 0;
#pragma unroll
      for (int i = 0; i < 8; ++i) { xv[i] -= mean; q2 += xv[i] * xv[i]; }
      const float rs = __builtin_amdgcn_rsqf(sum_all(q2) * (1.f / 512.f) + 1e-5f);
      if (g == j) lwr16(XN + wl * 320 + n * 16, u4{pk(xv[0] * rs, xv[1] * rs), pk(xv[2] * rs, xv[3] * rs), pk(xv[4] * rs, xv[5] * rs), pk(xv[6] * rs, xv[7] * rs)});
      STAMP(12);
      VMWAIT(0);
    }
    STAMP(1);
    bar();
    STAMP(2);
    EPOCH();
    {
      bf8 wa[4], xb[4];
#pragma unroll
      for (int s = 0; s < 4; ++s) {
        const int r0 = 32 * s + 8 * g + tq, r1 = r0 + 4;
        wa[s] = trfrag(r0 * 256 + ((w * 32 + tp * 8) ^ wsw(r0)), r1 * 256 + ((w * 32 + tp * 8) ^ wsw(r1)));
        xb[s] = asb(lrd16(XN + bb * 320 + s * 64 + g * 16));
      }
      SB();
      f4 a = {0, 0, 0, 0};
#pragma unroll
      for (int s = 0; s < 4; ++s) a = mfma16(wa[s], xb[s], a);
      if (v < 2) st16(ws, OFF_QP + cu * 4096 + (bb * 128 + w * 16 + 4 * g + 2 * v) * 8, v ? u4{asu(a[2]), elo, asu(a[3]), elo} : u4{asu(a[0]), elo, asu(a[1]), elo});
    }
    STAMP(3);
    bf8 uk[2][4];
    if (w >= 4) {
      u4 t[4];
      SLEEP(QP_DELAY);
#pragma nounroll
      for (int it = 0; it < SPIN_CAP; ++it) {
        FENCE();
        bool ok = true;
#pragma unroll
        for (int i = 0; i < 4; ++i) t[i] = ld16(ws, OFF_QP + (h * 4 + i) * 4096 + (tid - 256) * 16);
#pragma unroll
        for (int i = 0; i < 4; ++i) ok &= (t[i].y == elo && t[i].w == elo);
        if (__all(ok)) break;
        SLEEP(2);
      }
      lwr4(QB + wl * 320 + lane * 4, pk((asf(t[0].x) + asf(t[1].x)) + (asf(t[2].x) + asf(t[3].x)), (asf(t[0].z) + asf(t[1].z)) + (asf(t[2].z) + asf(t[3].z))));
    } else {
      if (w == 3) VMWAIT(2); else VMWAIT(1);
#pragma unroll
      for (int t = 0; t < 2; ++t)
#pragma unroll
        for (int s = 0; s < 4; ++s) uk[t][s] = asb(lrd16(32768 + w * 8192 + (8 * (n >> 2) + 4 * t + (n & 3)) * 256 + (((4 * s + g) ^ n) * 16)));
    }
    bar();
    STAMP(4);
    if (w < 4) {
      bf8 qb[4];
#pragma unroll
      for (int s = 0; s < 4; ++s) qb[s] = asb(lrd16(QB + bb * 320 + s * 64 + g * 16));
      SB();
      f4 a0 = {0, 0, 0, 0}, a1 = {0, 0, 0, 0};
#pragma unroll
      for (int s = 0; s < 4; ++s) { a0 = mfma16(uk[0][s], qb[s], a0); a1 = mfma16(uk[1][s], qb[s], a1); }
      const float sc = 0.12751721f;
      if (v == 0) st16(ws, OFF_Q + bb * 32768 + h * 1024 + j * 256 + w * 64 + g * 16,
                       u4{nosent(pk(a0[0] * sc, a0[1] * sc)), nosent(pk(a0[2] * sc, a0[3] * sc)), nosent(pk(a1[0] * sc, a1[1] * sc)), nosent(pk(a1[2] * sc, a1[3] * sc))});
    }
    bar();
    STAMP(5);
    if (TAIL_DELAY) SLEEP(TAIL_DELAY);
    const unsigned inb = OFF_IN + cu * IN_SZ;
    const u2 xres = *(const u2*)(P.x + bb * 1024 + cu * 8);
#pragma unroll
    for (int i = 0; i < 5; ++i) st16(ws, inb + i * 8192 + tid * 16, S4);
    {
      unsigned off = WS_USED;
      if (w == 1 && lane < 4 * RH) off = OFF_H + (lane >> 2) * 8192 + cu * 64 + bb * 16;
      if (w == 2 && lane < 6 * RHH) off = OFF_HH + cu * 96 * RHH + lane * 16;
      st16(ws, off, S4);
    }
    FENCE();
    {
      const u4 ru = mkrs(P.wuv + (size_t)(j * 128 + w * 16) * 8192 + h * 256, 0x20000);
      const unsigned v0 = g * 8192 + ((n * 16) ^ (g << 5));
#pragma unroll
      for (int i = 0; i < 4; ++i) blds(ru, v0 ^ ((i & 2) << 6), i * 32768, 131072 + w * 4096 + i * 1024);
      VMWAIT(4);
      bar();
      STAMP(13);
      if (tid < 8) st8(ws, OFF_F2 + tid * 1024 + cu * 8, u2{elo, ehi});
      const u4 ro = mkrs(P.wo + (size_t)h * 131072, 0x20000);
#pragma unroll
      for (int i = 0; i < 16; ++i) {
        const int row = ((i + WO_ROT * j) & 15) * 8 + w;
        WO_LD(ro, (lane * 16) ^ wsw(row), row * 1024, row * 1024);
      }
      const rsrc_t rf = __builtin_amdgcn_make_buffer_rsrc((void*)(w < 2 ? P.wg : w < 4 ? P.wu : P.wd), 0, 1409024, 0x00020000);
      const unsigned po = w < 4 ? (idx * 32 + (w & 1) * 16 + (lane >> 2)) * 2752 + xc * 384 + (bb < 3 ? bb * 128 : 380)
                                : ((w - 4) * 64 + lane < 86 ? (idx * 86 + (w - 4) * 64 + lane) * 1024 + xc * 128 : 1409024);
      const unsigned pf = __builtin_amdgcn_raw_buffer_load_b32(rf, po, 0, 0);
      VMWAIT(1); STAMP(14);
      asm volatile("" :: "v"(pf));
    }
    VMWAIT(0);
    bar();
    bf8 uv[4], wo[4][4];
#pragma unroll
    for (int s = 0; s < 4; ++s) {
      const int r0 = 32 * tq + 8 * s + 2 * g + (h & 1), r1 = r0 ^ 1;
      uv[s] = trfrag(131072 + r0 * 256 + ((w * 32 + tp * 8) ^ wsw(r0)), 131072 + r1 * 256 + ((w * 32 + tp * 8) ^ wsw(r1)));
    }
#pragma unroll
    for (int t = 0; t < 4; ++t)
#pragma unroll
      for (int s = 0; s < 4; ++s) {
        const int r0 = 32 * s + 8 * g + tq, r1 = r0 + 4;
        wo[t][s] = trfrag(r0 * 1024 + ((w * 128 + t * 32 + tp * 8) ^ wsw(r0)), r1 * 1024 + ((w * 128 + t * 32 + tp * 8) ^ wsw(r1)));
      }
    bar();
    STAMP(6);
    {
      u4 ml;
#pragma nounroll
      for (int it = 0; it < SPIN_CAP; ++it) {
        FENCE();
        ml = ld16(ws, OFF_ML + h * 2048 + (wl * 32 + (lane & 31)) * 16);
        if (__all(ml.y == elo && ml.w == elo)) break;
        SLEEP(ML_SLEEP);
      }
      FENCE();
      STAMP(7);
      u4 o[4];
      const unsigned oa = inb + wl * 8192 + (lane >> 3) * 256 + (w >> 2) * 128 + (lane & 7) * 16;
#pragma unroll
      for (int i = 0; i < 4; ++i) o[i] = ld16(ws, oa + i * 2048);
      float mx = asf(ml.x);
      mx = fmaxf(mx, dpp<0xB1>(mx)); mx = fmaxf(mx, dpp<0x4E>(mx)); mx = fmaxf(mx, dpp<0x124>(mx)); mx = fmaxf(mx, dpp<0x128>(mx)); mx = max16(mx);
      const float ex = __builtin_amdgcn_exp2f(asf(ml.x) - mx);
      float L = ex * asf(ml.z);
      L += dpp<0xB1>(L); L += dpp<0x4E>(L); L = add16(sum_v(L));
      const float wt = ex * __builtin_amdgcn_rcpf(L);
      float wi[4];
#pragma unroll
      for (int i = 0; i < 4; ++i) wi[i] = asf(__builtin_amdgcn_ds_bpermute((8 * i + (lane >> 3)) * 4, asu(wt)));
      for (int it = 0; it < SPIN_CAP; ++it) {
        if (__all(umax4(umax4(umax4(umax4(0, o[0]), o[1]), o[2]), o[3]) != SENT)) break;
        FENCE();
#pragma unroll
        for (int i = 0; i < 4; ++i) o[i] = ld16(ws, oa + i * 2048);
      }
      float acc[8] = {0, 0, 0, 0, 0, 0, 0, 0};
#pragma unroll
      for (int i = 0; i < 4; ++i) {
        acc[0] += wi[i] * bflo(o[i].x); acc[1] += wi[i] * bfhi(o[i].x); acc[2] += wi[i] * bflo(o[i].y); acc[3] += wi[i] * bfhi(o[i].y);
        acc[4] += wi[i] * bflo(o[i].z); acc[5] += wi[i] * bfhi(o[i].z); acc[6] += wi[i] * bflo(o[i].w); acc[7] += wi[i] * bfhi(o[i].w);
      }
      float t[4], u[2];
#pragma unroll
      for (int i = 0; i < 4; ++i) { auto s = __builtin_amdgcn_permlane32_swap(asu(acc[i]), asu(acc[i + 4]), false, false); t[i] = asf(s[0]) + asf(s[1]); }
#pragma unroll
      for (int i = 0; i < 2; ++i) { auto s = __builtin_amdgcn_permlane16_swap(asu(t[i]), asu(t[i + 2]), false, false); u[i] = asf(s[0]) + asf(s[1]); u[i] += dpp<0x128>(u[i]); }
      lwr4(CT + wl * 320 + (w >> 2) * 128 + (lane & 7) * 16 + g * 4, pk(u[0], u[1]));
    }
    bar();
    STAMP(8);
    {
      bf8 cb4[4];
#pragma unroll
      for (int s = 0; s < 4; ++s) cb4[s] = asb(lrd16(CT + bb * 320 + s * 64 + g * 16));
      SB();
      f4 a = {0, 0, 0, 0};
#pragma unroll
      for (int s = 0; s < 4; ++s) a = mfma16(uv[s], cb4[s], a);
      lwr8(CX + bb * 320 + (w * 16 + 4 * g) * 2, u2{pk(a[0], a[1]), pk(a[2], a[3])});
    }
    bar();
    {
      bf8 cbv[4];
#pragma unroll
      for (int s = 0; s < 4; ++s) cbv[s] = asb(lrd16(CX + bb * 320 + s * 64 + g * 16));
      SB();
      f4 a[4] = {{0, 0, 0, 0}, {0, 0, 0, 0}, {0, 0, 0, 0}, {0, 0, 0, 0}};
#pragma unroll
      for (int s = 0; s < 4; ++s)
#pragma unroll
        for (int t = 0; t < 4; ++t) a[t] = mfma16(wo[t][s], cbv[s], a[t]);
      const f4 r = sel4(v, a[0], a[1], a[2], a[3]);
      st16(ws, OFF_IN + IN_AP + (w * 16 + v * 4 + g) * IN_SZ + cu * 64 + bb * 16, u4{nosent(asu(r[0])), nosent(asu(r[1])), nosent(asu(r[2])), nosent(asu(r[3]))});
    }
    STAMP(9);
    if (cu == 0 && tid == 0) st8(ws, OFF_E, u2{elo, ehi});
    if (w == 7) {
      u4 t[8];
      for (int it = 0; it < SPIN_CAP; ++it) {
        FENCE();
        unsigned m = 0;
#pragma unroll
        for (int i = 0; i < 8; ++i) t[i] = ld16(ws, inb + IN_AP + i * 1024 + lane * 16);
#pragma unroll
        for (int i = 0; i < 8; ++i) m = umax4(m, t[i]);
        if (__all(m != SENT)) break;
      }
      f4 s = {0, 0, 0, 0};
#pragma unroll
      for (int i = 0; i < 8; ++i) s += __builtin_bit_cast(f4, t[i]);
      const float h0 = sum_vg(s[0]) + bflo(xres.x), h1 = sum_vg(s[1]) + bfhi(xres.x), h2 = sum_vg(s[2]) + bflo(xres.y), h3 = sum_vg(s[3]) + bfhi(xres.y);
      if (lane < 4 * RH) st16(ws, OFF_H + (lane >> 2) * 8192 + cu * 64 + bb * 16, u4{nosent(asu(h0)), nosent(asu(h1)), nosent(asu(h2)), nosent(asu(h3))});
    }
    STAMP(10);
  } else {
    const int b = xc >> 1;
    constexpr unsigned QL = 131072, PL = QL, MXL = QL + 8192, LSL = QL + 8192 + 1024;
    constexpr float TH = 40.f;
    if (w < 4) {
      lwr4(QL + (w * 8 + 7) * 1024 + lane * 16, SENT);
      bar();
      STAMP(1);
      __builtin_amdgcn_s_setprio(3);
      VMWAIT(F1POS);
      EPOCH();
#pragma nounroll
      for (int it = 0; it < SPIN_CAP; ++it) {
        FENCE();
        if (flag_ok(ld16(ws, OFF_F1 + xc * 1024 + lane * 16), elo, ehi)) break;
        SLEEP(16);
      }
      STAMP(2);
      const int krow = 4 * (n & 3) + (n >> 2);
      unsigned qkb = (w * 16 + krow) * 1024 + g * 256 + krow * 16;
      bf8 a[16];
      {
        if (Q_DELAY) SLEEP(Q_DELAY);
        u4 q[8];
        const unsigned qa = OFF_Q + b * 32768 + w * 8192 + lane * 16;
#pragma unroll
        for (int i = 0; i < 8; ++i) q[i] = ld16(ws, qa + i * 1024);
        FENCE();
#pragma unroll
        for (int s = 0; s < 16; ++s) a[s] = asb(lrd16(qkb ^ (s * 16)));
#pragma nounroll
        for (int it = 0; it < SPIN_CAP; ++it) {
          unsigned bad = 0;
#pragma unroll
          for (int i = 0; i < 8; ++i) bad |= __any(umax4(0, q[i]) == SENT) << i;
          if (!bad) break;
          FENCE();
#pragma unroll
          for (int i = 0; i < 8; ++i) if (bad >> i & 1) q[i] = ld16(ws, qa + i * 1024);
        }
#pragma unroll
        for (int i = 0; i < 8; ++i) { lwr16(QL + (w * 8 + i) * 1024 + ((lane ^ ((w * 8 + i) & 15)) * 16), q[i]); FENCE(); }
      }
      STAMP(3);
      __builtin_amdgcn_s_setprio(0);
#pragma nounroll
      for (int it = 0; it < SPIN_CAP; ++it) {
        FENCE();
        unsigned m = 0;
#pragma unroll
        for (int i = 0; i < 4; ++i) { const unsigned x = *(LAS unsigned*)(QL + (i * 8 + 7) * 1024 + lane * 16); m = x > m ? x : m; }
        if (__all(m != SENT)) break;
      }
      FENCE();
      u4 f2;
      bf8 qf[2][16];
#pragma unroll
      for (int s = 0; s < 16; ++s)
#pragma unroll
        for (int ht = 0; ht < 2; ++ht) qf[ht][s] = asb(lrd16(QL + (ht * 16 + n) * 1024 + g * 256 + ((s ^ n) * 16)));
      SB();
      float m0 = 0.f, m1 = 0.f, l0 = 0.f, l1 = 0.f;
      unsigned pa = PL + ((w * 2 + (g >> 1)) * 32 + n) * 16 + 8 * (g & 1), mxa = MXL;
      const unsigned lsa = LSL + n * 64 + (w * 4 + g) * 4;
#pragma unroll
      for (int r = 0; r < 4; ++r) {
        f4 s0 = {0, 0, 0, 0}, s1 = {0, 0, 0, 0};
#pragma unroll
        for (int s = 0; s < 16; ++s) { s0 = mfma16(a[s], qf[0][s], s0); s1 = mfma16(a[s], qf[1][s], s1); }
        SB();
        if (r == 0) bar();
        float t0, t1;
        auto emit = [&]() {
          f4 p0, p1;
#pragma unroll
          for (int i = 0; i < 4; ++i) { p0[i] = __builtin_amdgcn_exp2f(s0[i] - m0); p1[i] = __builtin_amdgcn_exp2f(s1[i] - m1); }
          lwr8(pa, u2{pk(p0[0], p0[1]), pk(p0[2], p0[3])});
          lwr8(pa + 256, u2{pk(p1[0], p1[1]), pk(p1[2], p1[3])});
          t0 = l0 + ((p0[0] + p0[1]) + (p0[2] + p0[3])); t1 = l1 + ((p1[0] + p1[1]) + (p1[2] + p1[3]));
          if (r == 3) { lwr4(lsa, asu(t0)); lwr4(lsa + 1024, asu(t1)); }
        };
        emit();
        {
          const float x0 = fmaxf(fmaxf(s0[0], s0[1]), fmaxf(s0[2], s0[3])), x1 = fmaxf(fmaxf(s1[0], s1[1]), fmaxf(s1[2], s1[3]));
          auto sw = __builtin_amdgcn_permlane16_swap(asu(x0), asu(x1), false, false);
          const float x = max32(fmaxf(asf(sw[0]), asf(sw[1])));
          lwr4(mxa + ((lane & 31) * 4 + w) * 4, asu(x));
        }
        if (r == 3 && w == 0) {
#pragma nounroll
          for (int it = 0; it < SPIN_CAP && !flag_ok(f2, elo, ehi); ++it) { FENCE(); f2 = ld16(ws, OFF_F2 + xc * 1024 + lane * 16); }
        }
        bar();
        if (r == 1) f2 = ld16(ws, w == 0 ? OFF_F2 + xc * 1024 + lane * 16 : WS_USED);
        if (r == 2) __builtin_amdgcn_s_setprio(3);
        const u4 ma = lrd16(mxa + n * 16), mb = lrd16(mxa + (16 + n) * 16);
        qkb ^= 65536;
        asm volatile("" : "+v"(qkb));
        if (r < 3) {
#pragma unroll
          for (int s = 0; s < 16; ++s) a[s] = asb(lrd16(qkb ^ (s * 16)));
        }
        SB();
        const float n0 = fmaxf(fmaxf(asf(ma.x), asf(ma.y)), fmaxf(asf(ma.z), asf(ma.w)));
        const float n1 = fmaxf(fmaxf(asf(mb.x), asf(mb.y)), fmaxf(asf(mb.z), asf(mb.w)));
        if (__builtin_expect(__any(r == 0 ? !(fabsf(n0) <= TH && fabsf(n1) <= TH) : (n0 > m0 + TH || n1 > m1 + TH)), 0)) {
          const float k0 = r == 0 ? n0 : fmaxf(m0, n0), k1 = r == 0 ? n1 : fmaxf(m1, n1);
          if (r > 0) { l0 *= __builtin_amdgcn_exp2f(m0 - k0); l1 *= __builtin_amdgcn_exp2f(m1 - k1); }
          m0 = k0; m1 = k1;
          bar();
          emit();
          bar();
        }
        l0 = t0; l1 = t1;
        pa ^= 4096; mxa ^= 512;
        if (r == 0 || r == 3) STAMP(5 + r);
      }
      if (w == 0) {
        f4 s = {0, 0, 0, 0};
#pragma unroll
        for (int i = 0; i < 4; ++i) s += __builtin_bit_cast(f4, lrd16(LSL + (lane & 31) * 64 + i * 16));
        if (lane < 32) st16(ws, OFF_ML + lane * 2048 + cu * 16, u4{asu(g ? m1 : m0), elo, asu((s[0] + s[1]) + (s[2] + s[3])), elo});
      }
      __builtin_amdgcn_s_setprio(0);
    } else {
      const int hf = lane >> 5, blk = g & 1;
      u4 Ld[32];
      const rsrc_t cr = __builtin_amdgcn_make_buffer_rsrc((void*)cb, 0, 262144, 0x00020000);
      const unsigned lp = (128 + g) * 1024 + wl * 256 + n * 16;
      bar();
#pragma unroll
      for (int i = 0; i < 32; ++i) { VMWAIT(PACE); Ld[i] = __builtin_amdgcn_raw_buffer_load_b128(cr, lp, i * 4096, AUX_NT); }
      VMWAIT(32);
      STAMP(1);
      f16v O[4];
#pragma unroll
      for (int c = 0; c < 4; ++c)
#pragma unroll
        for (int i = 0; i < 16; ++i) O[c][i] = 0.f;
      float mr = 0.f;
      unsigned trb[2];
#pragma unroll
      for (int t = 0; t < 2; ++t) { const int row = 4 * tq + 2 * hf + t; trb[t] = row * 1024 + wl * 256 + (((2 * blk + (tp >> 1)) ^ row) * 16) + 8 * (tp & 1); }
      const unsigned wb = g * 1024 + wl * 256 + ((n ^ g) * 16);
      bf8 a[4][4];
      auto rd = [&](int r, int ks) {
#pragma unroll
        for (int c = 0; c < 4; ++c) a[ks][c] = trfrag((r & 1) * 65536 + ks * 16384 + (trb[0] ^ (c * 64)), (r & 1) * 65536 + ks * 16384 + (trb[1] ^ (c * 64)));
      };
      bar();
      rd(0, 0);
#pragma unroll
      for (int r = 0; r < 4; ++r) {
        asm volatile("s_waitcnt lgkmcnt(8)\n\ts_barrier" ::: "memory");
        const u4 ma = lrd16(MXL + (r & 1) * 512 + (lane & 31) * 16);
        bf8 pb[4];
#pragma unroll
        for (int ks = 0; ks < (r ? 4 : 1); ++ks) pb[ks] = asb(lrd16(PL + (r & 1) * 4096 + (ks * 64 + lane) * 16));
        SB();
        const float n0 = fmaxf(fmaxf(asf(ma.x), asf(ma.y)), fmaxf(asf(ma.z), asf(ma.w)));
        if (__builtin_expect(__any(r == 0 ? !(fabsf(n0) <= TH) : n0 > mr + TH), 0)) {
          const float k0 = r == 0 ? n0 : fmaxf(mr, n0), al = __builtin_amdgcn_exp2f(mr - k0);
          mr = k0;
          bar();
          if (r > 0) {
#pragma unroll
            for (int i = 0; i < 16; ++i) {
              const float z = asf(__builtin_amdgcn_ds_bpermute((8 * (i >> 2) + 4 * hf + (i & 3)) * 4, asu(al)));
#pragma unroll
              for (int c = 0; c < 4; ++c) O[c][i] *= z;
            }
          }
          bar();
#pragma unroll
          for (int ks = 0; ks < (r ? 4 : 1); ++ks) pb[ks] = asb(lrd16(PL + (r & 1) * 4096 + (ks * 64 + lane) * 16));
        }
#pragma unroll
        for (int ks = 0; ks < 4; ++ks) {
          if (r < 2) {
            if (ks < 3) { rd(r, ks + 1); if (r == 0) pb[ks + 1] = asb(lrd16(PL + (ks * 64 + 64 + lane) * 16)); }
          }
          SB();
#pragma unroll
          for (int c = 0; c < 4; ++c) {
            O[c] = mfma32(pb[ks], a[ks][c], O[c]);
            if (r < 2) { SB(); lwr16((r & 1) * 65536 + (ks * 16 + 4 * c) * 1024 + (wb ^ (c * 64)), Ld[r * 16 + ks * 4 + c]); SB(); }
          }
          SB();
          if (r == 0 && ks == 3) rd(1, 0);
          if (r == 1 || r == 2) rd(r + 1, ks);
          SB();
        }
        if (r == 0 || r == 3) STAMP(5 + r);
      }
      const bool odd = lane & 1;
      const unsigned ob = OFF_IN + (hf * 16 + (lane & 1) * 4 + wl) * IN_SZ + cu * 256 + (lane & 30) * 8;
#pragma unroll
      for (int i = 0; i < 16; i += 2) {
        const unsigned a0 = nosent(pk(O[0][i], O[1][i])), a1 = nosent(pk(O[2][i], O[3][i]));
        const unsigned b0 = nosent(pk(O[0][i + 1], O[1][i + 1])), b1 = nosent(pk(O[2][i + 1], O[3][i + 1]));
        const unsigned y0 = __builtin_amdgcn_update_dpp(0u, odd ? a0 : b0, 0xB1, 0xF, 0xF, true), y1 = __builtin_amdgcn_update_dpp(0u, odd ? a1 : b1, 0xB1, 0xF, 0xF, true);
        __builtin_amdgcn_raw_buffer_store_b128(u4{odd ? b0 : a0, odd ? b1 : a1, y0, y1}, ws, ob, (8 * (i >> 2) + (i & 3)) * 4 * IN_SZ, AUX_ST);
      }
    }
    STAMP(9);
    const int fc = (idx >= 4 && idx < 10 && xc * 6 + idx - 4 < 43) ? xc * 6 + idx - 4 : -1;
    const int dc = idx < 4 ? xc * 4 + idx : -1;
    constexpr unsigned HL = 147456, RD = HL + 8192;
    if (fc >= 0) {
      bar();
      const char* src = ((lane & 4) ? P.wu : P.wg) + fc * 64 + (lane & 3) * 16 + (size_t)(w * 64 + (lane >> 3)) * 2752;
#pragma unroll
      for (int i = 0; i < 8; ++i) glds(src + i * 8 * 2752, (unsigned)((w * 64 + i * 8) * 128));
      VMWAIT(0);
      bar();
      bf8 ff[16];
      const unsigned a0 = (16 * g + tq) * 128 + (tp >> 1) * 64 + wl * 16 + (tp & 1) * 8;
#pragma unroll
      for (int k = 0; k < 16; ++k) ff[k] = trfrag(a0 + (128 * (k >> 2) + 4 * (k & 3)) * 128, a0 + (128 * (k >> 2) + 4 * (k & 3) + 64) * 128);
      STAMP(10);
      SLEEP(H_DELAY);
      {
        u4 t;
        for (int it = 0; it < SPIN_CAP; ++it) {
          FENCE();
          t = ld16(ws, OFF_H + (xc % RH) * 8192 + tid * 16);
          if (__all(umax4(0, t) != SENT)) break;
          SLEEP(1);
        }
        lwr16(HL + tid * 16, t);
      }
      bar();
      STAMP(11);
      if (w < 4) {
        f4 hv[8];
#pragma unroll
        for (int i = 0; i < 8; ++i) hv[i] = __builtin_bit_cast(f4, lrd16(HL + i * 1024 + lane * 16));
        SB();
        const f4 s4v = ((hv[0] + hv[1]) + (hv[2] + hv[3])) + ((hv[4] + hv[5]) + (hv[6] + hv[7]));
        const float mean = sum_vg((s4v[0] + s4v[1]) + (s4v[2] + s4v[3])) * (1.f / 512.f);
        f4 q4 = {0, 0, 0, 0};
        f4 a[4] = {{0, 0, 0, 0}, {0, 0, 0, 0}, {0, 0, 0, 0}, {0, 0, 0, 0}};
#pragma unroll
        for (int u = 0; u < 4; ++u) {
          const f4 y0 = hv[2 * u] - mean, y1 = hv[2 * u + 1] - mean;
          const bf8 hb = asb(u4{pk(y0[0], y0[1]), pk(y0[2], y0[3]), pk(y1[0], y1[1]), pk(y1[2], y1[3])});
#pragma unroll
          for (int vv = 0; vv < 4; ++vv) a[vv] = mfma16(ff[u * 4 + vv], hb, a[vv]);
          q4 += y0 * y0; q4 += y1 * y1;
        }
        const float rs = __builtin_amdgcn_rsqf(sum_vg((q4[0] + q4[1]) + (q4[2] + q4[3])) * (1.f / 512.f) + 1e-5f);
        const f4 r = sel4(v, a[0], a[1], a[2], a[3]);
        float hh[4];
#pragma unroll
        for (int i = 0; i < 4; ++i) {
          const float z = sum_v(r[i]) * rs;
          auto sw = __builtin_amdgcn_permlane32_swap(asu(z), asu(z), false, false);
          const float gt = asf(sw[0]), up = asf(sw[1]);
          const float inner = 0.7978845608f * (gt + 0.044715f * gt * gt * gt);
          hh[i] = gt * __builtin_amdgcn_rcpf(1.f + __builtin_amdgcn_exp2f(-2.885390082f * inner)) * up;
        }
        const unsigned p0 = nosent(pk(hh[0], hh[1])), p1 = nosent(pk(hh[2], hh[3]));
        auto s0 = __builtin_amdgcn_permlane16_swap(p0, p0, false, false);
        auto s1 = __builtin_amdgcn_permlane16_swap(p1, p1, false, false);
        if ((lane >> 2) < RHH) st16(ws, OFF_HH + (lane >> 2) * 12288 + (fc * 4 + w) * 64 + bb * 16, u4{s0[0], s1[0], s0[1], s1[1]});
      }
      STAMP(12);
    } else if (dc >= 0) {
      bar();
      for (int i = w; i < 43; i += 8) glds(P.wd + (size_t)(i * 32 + (lane >> 1)) * 1024 + dc * 32 + (lane & 1) * 16, (unsigned)(i * 1024));
      VMWAIT(0);
      bar();
      if (w < 4) {
        bf8 ff[12];
#pragma unroll
        for (int k = 0; k < 12; ++k) {
          int row = 8 * (16 * (3 * w + (k >> 2)) + 4 * g + (k & 3)) + tq; row = row > 1371 ? 1368 : row;
          ff[k] = trfrag(row * 32 + tp * 8, row * 32 + tp * 8 + 128);
        }
        STAMP(10);
        SLEEP(HH_DELAY);
        u4 t[3], hr;
        for (int it = 0; it < SPIN_CAP; ++it) {
          FENCE();
          hr = ld16(ws, OFF_H + (xc % RH) * 8192 + (dc * 4 + g) * 64 + bb * 16);
#pragma unroll
          for (int i = 0; i < 3; ++i) t[i] = ld16(ws, OFF_HH + (xc % RHH) * 12288 + (3 * w + i) * 1024 + lane * 16);
          unsigned m = umax4(0, hr);
#pragma unroll
          for (int i = 0; i < 3; ++i) { const bool live = (3 * w + i) * 16 + (lane >> 2) < 172; if (!live) t[i] = u4{0, 0, 0, 0}; m = umax4(m, t[i]); }
          if (__all(m != SENT)) break;
          SLEEP(1);
        }
        STAMP(13);
        f4 a[4] = {{0, 0, 0, 0}, {0, 0, 0, 0}, {0, 0, 0, 0}, {0, 0, 0, 0}};
#pragma unroll
        for (int i = 0; i < 3; ++i)
#pragma unroll
          for (int vv = 0; vv < 4; ++vv) a[vv] = mfma16(ff[i * 4 + vv], asb(t[i]), a[vv]);
        f4 r = sel4(v, a[0], a[1], a[2], a[3]);
#pragma unroll
        for (int i = 0; i < 4; ++i) r[i] = sum_v(r[i]);
        if (w == 0) r += __builtin_bit_cast(f4, hr);
        if (v == 0) lwr16(RD + w * 256 + (g * 4 + bb) * 16, __builtin_bit_cast(u4, r));
      }
      bar();
      if (w == 0 && v == 0) {
        f4 o[4];
#pragma unroll
        for (int i = 0; i < 4; ++i) o[i] = __builtin_bit_cast(f4, lrd16(RD + i * 256 + (g * 4 + bb) * 16));
        SB();
        const f4 z = (o[0] + o[1]) + (o[2] + o[3]);
        *(u2*)(P.out + bb * 1024 + dc * 32 + g * 8) = u2{pk(z[0], z[1]), pk(z[2], z[3])};
      }
      STAMP(14);
    }
  }
}

extern "C" void kernel_launch(void* const* d_in, const int* in_sizes, int n_in, void* d_out, int out_size, void* d_ws, size_t ws_size,
                              hipStream_t stream) {
  Params p;
  p.x = (const char*)d_in[0]; p.ckv = (const char*)d_in[1]; p.wq = (const char*)d_in[2]; p.wuk = (const char*)d_in[3];
  p.wuv = (const char*)d_in[4]; p.wo = (const char*)d_in[5]; p.wg = (const char*)d_in[7]; p.wu = (const char*)d_in[8];
  p.wd = (const char*)d_in[9]; p.out = (char*)d_out; p.ws = (char*)d_ws;
  (void)hipFuncSetAttribute((const void*)fused, hipFuncAttributeMaxDynamicSharedMemorySize, 163840);
  fused<<<256, 512, 163840, stream>>>(p);
}
```
